```python
import torch

def _softmax(x, axis=-1):
    return torch.softmax(x, dim=axis)
def _gelu(x):
    return torch.nn.functional.gelu(x, approximate="tanh")
def _silu(x):
    return torch.nn.functional.silu(x)
def _sigmoid(x):
    return torch.sigmoid(x)
def _layernorm(x, axis=-1, eps=1e-5):
    m = x.mean(dim=axis, keepdim=True)
    v = x.var(dim=axis, keepdim=True, unbiased=False)
    return (x - m) / torch.sqrt(v + eps)
def _scatter_add(tgt, idx, src, axis=0):
    return tgt.clone().index_add_(axis, idx, src)
def _segment_sum(src, seg, n):
    out = torch.zeros((n,) + src.shape[1:], dtype=src.dtype)
    return out.index_add_(0, seg, src)
def _pad(x, pw):
    flat = tuple(v for pair in reversed(pw) for v in pair)
    return torch.nn.functional.pad(x, flat)

kernel_name = 'concat_heads__gqa_block_cached__sliding_block_cached__decode'

def setup_inputs():

    _w = (lambda a: (lambda t: t.to(torch.bfloat16) if t.is_floating_point() else t)(torch.as_tensor(a)))
    def _A_setup():
        B, Sq, D = 4, 1, 1024
        Hq, Hkv, Sk, Dh = 32, 4, 8192, 128
        assert Hq % Hkv == 0, (Hq, Hkv)
        return {
            "x": _w((lambda *s: torch.randn(*s, dtype=torch.bfloat16) * 0.8)(B, Sq, D)),
            "k_cache": _w((lambda *s: torch.randn(*s, dtype=torch.bfloat16) * 1.6)(B, Hkv, Sk, Dh)),
            "v_cache": _w((lambda *s: torch.randn(*s, dtype=torch.bfloat16) * 0.2)(B, Hkv, Sk, Dh)),
            "wq": _w((lambda *s: torch.randn(*s, dtype=torch.bfloat16) * 1.6)(D, Hq * Dh) * (D ** -0.5)),
            "wo": _w((lambda *s: torch.randn(*s, dtype=torch.bfloat16) * 0.2)(Hq * Dh, D) * ((Hq * Dh) ** -0.5)),
        }
    _A_in = _A_setup()

    def _B_setup():
        B, Sq, D = 4, 1, 1024
        Hq, Hkv, Sk, Dh = 32, 4, 8192, 128
        assert Hq % Hkv == 0, (Hq, Hkv)
        return {
            "x": _w((lambda *s: torch.randn(*s, dtype=torch.bfloat16) * 0.8)(B, Sq, D)),
            "k_cache": _w((lambda *s: torch.randn(*s, dtype=torch.bfloat16) * 1.6)(B, Hkv, Sk, Dh)),
            "v_cache": _w((lambda *s: torch.randn(*s, dtype=torch.bfloat16) * 0.2)(B, Hkv, Sk, Dh)),
            "wq": _w((lambda *s: torch.randn(*s, dtype=torch.bfloat16) * 1.6)(D, Hq * Dh) * (D ** -0.5)),
            "wo": _w((lambda *s: torch.randn(*s, dtype=torch.bfloat16) * 0.2)(Hq * Dh, D) * ((Hq * Dh) ** -0.5)),
        }
    _B_in = _B_setup()

    return {
        "A_x": _A_in["x"],
        "A_k_cache": _A_in["k_cache"],
        "A_v_cache": _A_in["v_cache"],
        "A_wq": _A_in["wq"],
        "A_wo": _A_in["wo"],
        "B_x": _B_in["x"],
        "B_k_cache": _B_in["k_cache"],
        "B_v_cache": _B_in["v_cache"],
        "B_wq": _B_in["wq"],
        "B_wo": _B_in["wo"],
    }

def reference(A_x, A_k_cache, A_v_cache, A_wq, A_wo, B_x, B_k_cache, B_v_cache, B_wq, B_wo):
    def _A_ref(x, k_cache, v_cache, wq, wo):
        B, Sq, D = x.shape
        Hq, Dh = 32, 128
        Hkv = k_cache.shape[1]
        rep = Hq // Hkv
        q = (lambda x, *d: x.permute(*d))((x @ wq).reshape(B, Sq, Hq, Dh), 0, 2, 1, 3)
        k = (lambda x, n, axis: torch.repeat_interleave(x, n, dim=axis))(k_cache, rep, axis=1)
        v = (lambda x, n, axis: torch.repeat_interleave(x, n, dim=axis))(v_cache, rep, axis=1)
        scores = torch.einsum("bhqd,bhkd->bhqk", q, k) * (Dh ** -0.5)
        p = _softmax(scores, axis=-1)
        o = torch.einsum("bhqk,bhkd->bhqd", p, v)
        return (lambda x, *d: x.permute(*d))(o, 0, 2, 1, 3).reshape(B, Sq, Hq * Dh) @ wo

    def _B_ref(x, k_cache, v_cache, wq, wo):
        B, Sq, D = x.shape
        Hq, Dh = 32, 128
        Hkv, Sk = k_cache.shape[1], k_cache.shape[2]
        W = min(4096, max(256, Sk // 4))
        rep = Hq // Hkv
        q = (lambda x, *d: x.permute(*d))((x @ wq).reshape(B, Sq, Hq, Dh), 0, 2, 1, 3)
        k = (lambda x, n, axis: torch.repeat_interleave(x, n, dim=axis))(k_cache[:, :, Sk - W:, :], rep, axis=1)
        v = (lambda x, n, axis: torch.repeat_interleave(x, n, dim=axis))(v_cache[:, :, Sk - W:, :], rep, axis=1)
        scores = torch.einsum("bhqd,bhkd->bhqk", q, k) * (Dh ** -0.5)
        p = _softmax(scores, axis=-1)
        o = torch.einsum("bhqk,bhkd->bhqd", p, v)
        return (lambda x, *d: x.permute(*d))(o, 0, 2, 1, 3).reshape(B, Sq, Hq * Dh) @ wo

    a = _A_ref(A_x, A_k_cache, A_v_cache, A_wq, A_wo)
    b = _B_ref(B_x, B_k_cache, B_v_cache, B_wq, B_wo)
    return torch.concatenate([a, b], axis=-1)
```

```cpp
#include <hip/hip_runtime.h>
#include <stdint.h>

#define KV_NT 1
#define W_NT 1
#define STAMPS 0

typedef unsigned v4u __attribute__((ext_vector_type(4)));
typedef unsigned v2u __attribute__((ext_vector_type(2)));
typedef float f32x4 __attribute__((ext_vector_type(4)));
typedef __bf16 bf16x8 __attribute__((ext_vector_type(8)));
typedef short s16x4 __attribute__((ext_vector_type(4)));
#define GAS __attribute__((address_space(1)))
#define LAS __attribute__((address_space(3)))
#define SB() __builtin_amdgcn_sched_barrier(0)

constexpr int WS_EPOCH = 0;
constexpr int WS_QG = 4096;
constexpr int WS_ML = WS_QG + 2 * 4 * 4 * 1024 * 8;
constexpr int WS_PO = WS_ML + 2 * 32 * 64 * 8;
constexpr int WS_OUT = WS_PO + 2 * 32 * 64 * 128 * 4;
constexpr int WS_END = WS_OUT + 256 * 32 * 32 * 4;
constexpr int WS_STAMP = WS_END;
constexpr int AUX_SC1 = 16;
constexpr unsigned SENT = 0xFFFFFFFFu;
constexpr unsigned SPIN_CAP = 1u << 16;

constexpr int L_QRED = 0;
constexpr int L_QS = L_QRED + 16384;
constexpr int L_MX = L_QS + 2 * 8 * 272;
constexpr int L_LS = L_MX + 512;
constexpr int L_RED = L_LS + 512;
constexpr int L_OB = L_RED + 2 * 8 * 8 * 528;
constexpr int L_RED2 = L_OB + 4096;
constexpr int L_END = L_RED2 + 4 * 4 * 1040;

template <bool NT> __device__ __forceinline__ v4u ldg(const char* p) {
  if (NT) return __builtin_nontemporal_load((const GAS v4u*)p);
  else return *(const GAS v4u*)p;
}
__device__ __forceinline__ f32x4 mfma32(v4u a, v4u b, f32x4 c) {
  return __builtin_amdgcn_mfma_f32_16x16x32_bf16(__builtin_bit_cast(bf16x8, a), __builtin_bit_cast(bf16x8, b), c, 0, 0, 0);
}
__device__ __forceinline__ f32x4 mfma16(v2u a, v2u b, f32x4 c) {
  return __builtin_amdgcn_mfma_f32_16x16x16bf16_1k(__builtin_bit_cast(s16x4, a), __builtin_bit_cast(s16x4, b), c, 0, 0, 0);
}
__device__ __forceinline__ unsigned pk_bf16(float lo, float hi) {
  typedef __bf16 bf16x2 __attribute__((ext_vector_type(2)));
  typedef float f32x2 __attribute__((ext_vector_type(2)));
  f32x2 v = {lo, hi};
  return __builtin_bit_cast(unsigned, __builtin_convertvector(v, bf16x2));
}
__device__ __forceinline__ unsigned tr2(const v4u& r0, const v4u& r1, int dd) {
  return __builtin_amdgcn_perm(r1[dd >> 1], r0[dd >> 1], (dd & 1) ? 0x07060302u : 0x05040100u);
}
__device__ __forceinline__ float xg_max(float x) {
  auto s = __builtin_amdgcn_permlane16_swap(__float_as_uint(x), __float_as_uint(x), false, false);
  x = fmaxf(__uint_as_float(s[0]), __uint_as_float(s[1]));
  auto t = __builtin_amdgcn_permlane32_swap(__float_as_uint(x), __float_as_uint(x), false, false);
  return fmaxf(__uint_as_float(t[0]), __uint_as_float(t[1]));
}
__device__ __forceinline__ float x16_sum(float x) {
  auto s = __builtin_amdgcn_permlane16_swap(__float_as_uint(x), __float_as_uint(x), false, false);
  return __uint_as_float(s[0]) + __uint_as_float(s[1]);
}
__device__ __forceinline__ float x32_sum(float x) {
  auto t = __builtin_amdgcn_permlane32_swap(__float_as_uint(x), __float_as_uint(x), false, false);
  return __uint_as_float(t[0]) + __uint_as_float(t[1]);
}
template <int CTRL> __device__ __forceinline__ float dpp(float x) {
  return __builtin_bit_cast(float, __builtin_amdgcn_mov_dpp(__builtin_bit_cast(int, x), CTRL, 0xf, 0xf, true));
}
constexpr int ROR1 = 0x121, ROR2 = 0x122, ROR4 = 0x124, ROR8 = 0x128;
__device__ __forceinline__ v4u nosent(f32x4 v) {
  v4u u = __builtin_bit_cast(v4u, v);
  u.x = min(u.x, SENT - 1u); u.y = min(u.y, SENT - 1u); u.z = min(u.z, SENT - 1u); u.w = min(u.w, SENT - 1u);
  return u;
}
__device__ __forceinline__ bool has_sent(v4u u) { return max(max(u.x, u.y), max(u.z, u.w)) == SENT; }
__device__ __forceinline__ void lds_barrier() {
  asm volatile("s_waitcnt lgkmcnt(0)\n\ts_barrier" ::: "memory");
}
template <int N> __device__ __forceinline__ void waitvm() { asm volatile("s_waitcnt vmcnt(%0)" ::"i"(N) : "memory"); }

#define STAMP(n) do {} while (0)

__global__ void __launch_bounds__(512, 2)
fused_decode(const char* __restrict__ xA, const char* __restrict__ kA, const char* __restrict__ vA, const char* __restrict__ wqA,
             const char* __restrict__ woA, const char* __restrict__ xB, const char* __restrict__ kB, const char* __restrict__ vB,
             const char* __restrict__ wqB, const char* __restrict__ woB, unsigned short* __restrict__ out, char* __restrict__ ws) {
  __shared__ __attribute__((aligned(16))) char smem[L_END];
  LAS char* L = (LAS char*)smem;
  const int c = blockIdx.x, tid = threadIdx.x, l = tid & 63, w = __builtin_amdgcn_readfirstlane(tid >> 6);
  const int idx = (c & 7) * 32 + (c >> 3), kvh = idx >> 6, gi = idx & 63;
  const int br = gi >> 5, cb = gi & 31;
  const int b = gi >> 4, s = gi & 15;
  const int h = kvh * 8 + ((gi >> 2) & 7), cq = gi & 3;
  const int g = l >> 4, rho = l & 15;
  const __amdgpu_buffer_rsrc_t rs = __builtin_amdgcn_make_buffer_rsrc((void*)ws, 0, WS_END, 0x00020000);
  STAMP(0);

  const unsigned long long epoch =
      __hip_atomic_load((GAS unsigned long long*)(ws + WS_EPOCH), __ATOMIC_RELAXED, __HIP_MEMORY_SCOPE_AGENT);
  if (w == 0) {
    __builtin_amdgcn_s_setprio(3);
    const v4u sent = {SENT, SENT, SENT, SENT};
    const int po = WS_PO + ((br * 32 + h) * 64 + cq * 16) * 512 + l * 16;
#pragma unroll
    for (int n = 0; n < 8; ++n) __builtin_amdgcn_raw_buffer_store_b128(sent, rs, po + n * 1024, 0, AUX_SC1);
    const int oo = WS_OUT + idx * 4096 + l * 16;
#pragma unroll
    for (int n = 0; n < 4; ++n) __builtin_amdgcn_raw_buffer_store_b128(sent, rs, oo + n * 1024, 0, AUX_SC1);
    if (l < 8) __builtin_amdgcn_raw_buffer_store_b128(sent, rs, WS_ML + ((br * 32 + h) * 64 + cq * 16) * 8 + l * 16, 0, AUX_SC1);
  }
  SB();

  const v4u xf = *(const GAS v4u*)((br ? xB : xA) + ((l & 3) * 1024 + 128 * w + 8 * (l >> 2)) * 2);
  v4u wr[8];
  {
    const char* p = (br ? wqB : wqA) + (size_t)(128 * w + 8 * (l >> 2)) * 8192 + (kvh * 1024 + cb * 32) * 2 + (l & 3) * 16;
#pragma unroll
    for (int n = 0; n < 8; ++n) wr[n] = ldg<W_NT>(p + n * 8192);
  }
  SB();
  const size_t pairoff = (size_t)(b * 4 + kvh) * (8192 * 256);
  const int kap = 4 * (rho & 3) + (rho >> 2);
  const size_t offB = pairoff + (size_t)(6144 + 128 * s + 16 * w) * 256, offA = pairoff + (size_t)(512 * s + 64 * w) * 256;
  v4u kf[20], vf[20];
  {
    const char* p = kB + offB + kap * 256 + g * 16;
#pragma unroll
    for (int n = 0; n < 4; ++n) kf[n] = ldg<KV_NT>(p + n * 64);
    const char* q = kA + offA + kap * 256 + g * 16;
#pragma unroll
    for (int n = 0; n < 16; ++n) kf[4 + n] = ldg<KV_NT>(q + (n >> 2) * 4096 + (n & 3) * 64);
  }
  SB();

  {
    f32x4 qa[8];
#pragma unroll
    for (int dd = 0; dd < 8; ++dd) {
      const v4u a = {tr2(wr[0], wr[1], dd), tr2(wr[2], wr[3], dd), tr2(wr[4], wr[5], dd), tr2(wr[6], wr[7], dd)};
      qa[dd] = mfma32(a, xf, f32x4{0.f, 0.f, 0.f, 0.f});
    }
    if (((l >> 2) & 3) == g) {
      LAS f32x4* d = (LAS f32x4*)(L + L_QRED + ((w * 4 + g) * 4 + (l & 3)) * 128);
#pragma unroll
      for (int dd = 0; dd < 8; ++dd) d[dd] = qa[dd];
    }
  }
  STAMP(1);
  lds_barrier();
  SB();

  if (w == 0) {
    const LAS f32x4* src = (const LAS f32x4*)(L + L_QRED) + (l >> 5) * 16 * 32 + (l & 31);
    f32x4 a = src[0];
#pragma unroll
    for (int n = 1; n < 16; ++n) a += src[n * 32];
    a.x = x32_sum(a.x); a.y = x32_sum(a.y); a.z = x32_sum(a.z); a.w = x32_sum(a.w);
    const unsigned long long tag = (epoch + 1ull) << 16;
    const unsigned tlo = (unsigned)tag, thi = (unsigned)(tag >> 32);
    const unsigned p01 = pk_bf16(a.x, a.y), p23 = pk_bf16(a.z, a.w);
    const unsigned pp = (l >> 5) ? p23 : p01;
    const int r0 = (l >> 5) * 2;
    const int qbase = WS_QG + (((br * 4 + kvh) * 4 + ((l & 31) >> 3)) * 1024 + cb * 32 + (l & 7)) * 8;
    waitvm<0>();
    __builtin_amdgcn_raw_buffer_store_b64(v2u{tlo | (pp & 0xFFFFu), thi}, rs, qbase + r0 * 64, 0, AUX_SC1);
    __builtin_amdgcn_raw_buffer_store_b64(v2u{tlo | (pp >> 16), thi}, rs, qbase + r0 * 64 + 64, 0, AUX_SC1);
    STAMP(2);
    const int gA = WS_QG + ((kvh * 4 + b) * 1024) * 8 + l * 16, gB = gA + 16 * 1024 * 8;
    const int fj = (br * 4 + cq) * 32 + (l & 31);
    const int fo = WS_QG + (((((fj >> 5) & 1) * 4 + (fj >> 6)) * 4) * 1024 + (fj & 31) * 32) * 8;
    v4u qg[16];
    for (unsigned spin = 0;; ++spin) {
      asm volatile("" ::: "memory");
#pragma unroll
      for (int n = 0; n < 8; ++n) qg[n] = __builtin_amdgcn_raw_buffer_load_b128(rs, gA + n * 1024, 0, AUX_SC1);
#pragma unroll
      for (int n = 0; n < 8; ++n) qg[8 + n] = __builtin_amdgcn_raw_buffer_load_b128(rs, gB + n * 1024, 0, AUX_SC1);
      const v2u fl = __builtin_amdgcn_raw_buffer_load_b64(rs, fo, 0, AUX_SC1);
      unsigned bad = ((fl.x ^ tlo) & 0xFFFF0000u) | (fl.y ^ thi);
#pragma unroll
      for (int n = 0; n < 16; ++n)
        bad |= (((qg[n].x ^ tlo) | (qg[n].z ^ tlo)) & 0xFFFF0000u) | (qg[n].y ^ thi) | (qg[n].w ^ thi);
      if (__builtin_amdgcn_ballot_w64(bad != 0) == 0ull || spin > SPIN_CAP) break;
      __builtin_amdgcn_s_sleep(2);
    }
#pragma unroll
    for (int n = 0; n < 16; ++n)
      *(LAS unsigned*)(L + L_QS + n * 272 + l * 4) = (qg[n].x & 0xFFFFu) | (qg[n].z << 16);
    STAMP(3);
  }
  SB();
  {
    const char* p = vB + offB + l * 16;
#pragma unroll
    for (int n = 0; n < 4; ++n) vf[n] = ldg<KV_NT>(p + n * 1024);
    const char* q = vA + offA + l * 16;
#pragma unroll
    for (int n = 0; n < 16; ++n) vf[4 + n] = ldg<KV_NT>(q + n * 1024);
  }
  SB();
  STAMP(4);
  lds_barrier();
  SB();

  constexpr float C2 = 0.08838834764831845f * 1.4426950408889634f;
  f32x4 sc[5];
  {
    const LAS char* qp = L + L_QS + (l & 7) * 272 + g * 16;
    f32x4 a = {0.f, 0.f, 0.f, 0.f};
#pragma unroll
    for (int t = 0; t < 4; ++t) a = mfma32(kf[t], *(const LAS v4u*)(qp + 8 * 272 + t * 64), a);
    sc[0] = a;
    v4u qf[4];
#pragma unroll
    for (int t = 0; t < 4; ++t) qf[t] = *(const LAS v4u*)(qp + t * 64);
#pragma unroll
    for (int u = 0; u < 4; ++u) {
      f32x4 z = {0.f, 0.f, 0.f, 0.f};
#pragma unroll
      for (int t = 0; t < 4; ++t) z = mfma32(kf[4 + 4 * u + t], qf[t], z);
      sc[1 + u] = z;
    }
  }
  SB();
  v4u wo[8];
  {
    const char* p = (br ? woB : woA) + (size_t)(128 * h + 32 * (w >> 1) + 2 * g) * 2048 + (256 * cq + 128 * (w & 1)) * 2 + rho * 16;
#pragma unroll
    for (int j = 0; j < 8; ++j) wo[j] = ldg<W_NT>(p + (8 * (j & 3) + (j >> 2)) * 2048);
  }
  SB();
  {
    float mB = fmaxf(fmaxf(sc[0].x, sc[0].y), fmaxf(sc[0].z, sc[0].w));
    float mA = fmaxf(fmaxf(sc[1].x, sc[1].y), fmaxf(sc[1].z, sc[1].w));
#pragma unroll
    for (int u = 2; u < 5; ++u) mA = fmaxf(mA, fmaxf(fmaxf(sc[u].x, sc[u].y), fmaxf(sc[u].z, sc[u].w)));
    mA = xg_max(mA); mB = xg_max(mB);
    if (l < 8) {
      *(LAS float*)(L + L_MX + (l * 8 + w) * 4) = mA;
      *(LAS float*)(L + L_MX + 256 + (l * 8 + w) * 4) = mB;
    }
  }
  STAMP(5);
  lds_barrier();
  float MA, MB;
  {
    const LAS f32x4* m = (const LAS f32x4*)(L + L_MX + (l & 7) * 32);
    const f32x4 a0 = m[0], a1 = m[1], b0 = m[16], b1 = m[17];
    MA = fmaxf(fmaxf(fmaxf(a0.x, a0.y), fmaxf(a0.z, a0.w)), fmaxf(fmaxf(a1.x, a1.y), fmaxf(a1.z, a1.w))) * C2;
    MB = fmaxf(fmaxf(fmaxf(b0.x, b0.y), fmaxf(b0.z, b0.w)), fmaxf(fmaxf(b1.x, b1.y), fmaxf(b1.z, b1.w))) * C2;
  }
  unsigned pf[10];
  {
    float la = 0.f, lb = 0.f;
#pragma unroll
    for (int u = 0; u < 5; ++u) {
      const float M = u ? MA : MB;
      const float p0 = __builtin_amdgcn_exp2f(fmaf(sc[u].x, C2, -M)), p1 = __builtin_amdgcn_exp2f(fmaf(sc[u].y, C2, -M));
      const float p2 = __builtin_amdgcn_exp2f(fmaf(sc[u].z, C2, -M)), p3 = __builtin_amdgcn_exp2f(fmaf(sc[u].w, C2, -M));
      const float t = (p0 + p1) + (p2 + p3);
      if (u) la += t; else lb = t;
      pf[2 * u] = pk_bf16(p0, p1); pf[2 * u + 1] = pk_bf16(p2, p3);
    }
    la = x32_sum(x16_sum(la)); lb = x32_sum(x16_sum(lb));
    if (l < 8) {
      *(LAS float*)(L + L_LS + (l * 8 + w) * 4) = la;
      *(LAS float*)(L + L_LS + 256 + (l * 8 + w) * 4) = lb;
    }
  }
  SB();
  {
    LAS f32x4* dB = (LAS f32x4*)(L + L_RED + 8 * 8 * 528 + (w * 8 + (l & 7)) * 528 + g * 128);
    f32x4 acc[8];
#pragma unroll
    for (int dd = 0; dd < 8; ++dd) {
      const v2u a = {tr2(vf[0], vf[1], dd), tr2(vf[2], vf[3], dd)};
      acc[dd] = mfma16(a, v2u{pf[0], pf[1]}, f32x4{0.f, 0.f, 0.f, 0.f});
    }
    if (rho < 8) {
#pragma unroll
      for (int dd = 0; dd < 8; ++dd) dB[dd] = acc[dd];
    }
  }
  SB();
  {
    LAS f32x4* dA = (LAS f32x4*)(L + L_RED + (w * 8 + (l & 7)) * 528 + g * 128);
    f32x4 acc[8];
#pragma unroll
    for (int dd = 0; dd < 8; ++dd) {
      const v4u a = {tr2(vf[4], vf[5], dd), tr2(vf[6], vf[7], dd), tr2(vf[8], vf[9], dd), tr2(vf[10], vf[11], dd)};
      acc[dd] = mfma32(a, v4u{pf[2], pf[3], pf[4], pf[5]}, f32x4{0.f, 0.f, 0.f, 0.f});
    }
    SB();
#pragma unroll
    for (int dd = 0; dd < 8; ++dd) {
      const v4u a = {tr2(vf[12], vf[13], dd), tr2(vf[14], vf[15], dd), tr2(vf[16], vf[17], dd), tr2(vf[18], vf[19], dd)};
      acc[dd] = mfma32(a, v4u{pf[6], pf[7], pf[8], pf[9]}, acc[dd]);
    }
    if (rho < 8) {
#pragma unroll
      for (int dd = 0; dd < 8; ++dd) dA[dd] = acc[dd];
    }
  }
  STAMP(6);
  lds_barrier();
  {
    const int pb = tid >> 8, hd = (tid >> 5) & 7, quad = tid & 31;
    const LAS char* src = L + L_RED + pb * (8 * 8 * 528) + hd * 528 + quad * 16;
    f32x4 a = *(const LAS f32x4*)src;
#pragma unroll
    for (int ww = 1; ww < 8; ++ww) a += *(const LAS f32x4*)(src + ww * (8 * 528));
    const int rec = (pb * 32 + kvh * 8 + hd) * 64 + b * 16 + s;
    __builtin_amdgcn_raw_buffer_store_b128(nosent(a), rs, WS_PO + rec * 512 + quad * 16, 0, AUX_SC1);
    if (quad == 0) {
      const LAS f32x4* ls = (const LAS f32x4*)(L + L_LS + pb * 256 + hd * 32);
      const LAS f32x4* mx = (const LAS f32x4*)(L + L_MX + pb * 256 + hd * 32);
      const f32x4 l0 = ls[0], l1 = ls[1], m0 = mx[0], m1 = mx[1];
      const float lsum = ((l0.x + l0.y) + (l0.z + l0.w)) + ((l1.x + l1.y) + (l1.z + l1.w));
      const float mm = fmaxf(fmaxf(fmaxf(m0.x, m0.y), fmaxf(m0.z, m0.w)), fmaxf(fmaxf(m1.x, m1.y), fmaxf(m1.z, m1.w))) * C2;
      __builtin_amdgcn_raw_buffer_store_b64(v2u{min(__float_as_uint(mm), SENT - 1u), min(__float_as_uint(lsum), SENT - 1u)}, rs,
                                            WS_ML + rec * 8, 0, AUX_SC1);
    }
  }
  STAMP(7);
  SB();

  {
    const int rec0 = (br * 32 + h) * 64 + (w >> 1) * 16;
    const int po = WS_PO + (rec0 + 8 * (w & 1) + (l >> 5)) * 512 + (l & 31) * 16;
    const int mo = WS_ML + (rec0 + rho) * 8;
    v4u pv[4]; v2u ml;
    for (unsigned spin = 0;; ++spin) {
      asm volatile("" ::: "memory");
#pragma unroll
      for (int n = 0; n < 4; ++n) pv[n] = __builtin_amdgcn_raw_buffer_load_b128(rs, po + n * 1024, 0, AUX_SC1);
      ml = __builtin_amdgcn_raw_buffer_load_b64(rs, mo, 0, AUX_SC1);
      const bool bad = has_sent(pv[0]) | has_sent(pv[1]) | has_sent(pv[2]) | has_sent(pv[3]) | (max(ml.x, ml.y) == SENT);
      if (__builtin_amdgcn_ballot_w64(bad) == 0ull || spin > SPIN_CAP) break;
      __builtin_amdgcn_s_sleep(2);
    }
    STAMP(8);
    const float m = __uint_as_float(ml.x), ls = __uint_as_float(ml.y);
    float M = m;
    M = fmaxf(M, dpp<ROR8>(M)); M = fmaxf(M, dpp<ROR4>(M)); M = fmaxf(M, dpp<ROR2>(M)); M = fmaxf(M, dpp<ROR1>(M));
    const float e = __builtin_amdgcn_exp2f(m - M);
    float Ls = e * ls;
    Ls += dpp<ROR8>(Ls); Ls += dpp<ROR4>(Ls); Ls += dpp<ROR2>(Ls); Ls += dpp<ROR1>(Ls);
    const float wg = e / Ls;
    f32x4 o = {0.f, 0.f, 0.f, 0.f};
#pragma unroll
    for (int n = 0; n < 4; ++n) {
      const float wlo = __builtin_bit_cast(float, __builtin_amdgcn_readlane(__builtin_bit_cast(int, wg), 8 * (w & 1) + 2 * n));
      const float whi = __builtin_bit_cast(float, __builtin_amdgcn_readlane(__builtin_bit_cast(int, wg), 8 * (w & 1) + 2 * n + 1));
      const float ww = (l >> 5) ? whi : wlo;
      o += __builtin_bit_cast(f32x4, pv[n]) * ww;
    }
    o.x = x32_sum(o.x); o.y = x32_sum(o.y); o.z = x32_sum(o.z); o.w = x32_sum(o.w);
    if (l < 32) *(LAS f32x4*)(L + L_OB + w * 512 + l * 16) = o;
  }
  lds_barrier();
  {
    const LAS char* src = L + L_OB + (l & 3) * 1024 + (32 * (w >> 1) + 8 * g) * 4;
    const f32x4 o0 = *(const LAS f32x4*)src + *(const LAS f32x4*)(src + 512);
    const f32x4 o1 = *(const LAS f32x4*)(src + 16) + *(const LAS f32x4*)(src + 528);
    const v4u of = {pk_bf16(o0.x, o0.y), pk_bf16(o0.z, o0.w), pk_bf16(o1.x, o1.y), pk_bf16(o1.z, o1.w)};
    LAS f32x4* d = (LAS f32x4*)(L + L_RED2 + ((w >> 1) * 4 + (l & 3)) * 1040 + (128 * (w & 1) + 32 * g) * 4);
    f32x4 acc[8];
#pragma unroll
    for (int dd = 0; dd < 8; ++dd) {
      const v4u a = {tr2(wo[0], wo[1], dd), tr2(wo[2], wo[3], dd), tr2(wo[4], wo[5], dd), tr2(wo[6], wo[7], dd)};
      acc[dd] = mfma32(a, of, f32x4{0.f, 0.f, 0.f, 0.f});
    }
    if (rho < 4) {
#pragma unroll
      for (int dd = 0; dd < 8; ++dd) d[dd] = acc[dd];
    }
  }
  lds_barrier();
  if (tid < 256) {
    const int bb = tid >> 6, quad = tid & 63;
    const LAS char* src = L + L_RED2 + bb * 1040 + quad * 16;
    const f32x4 a = (*(const LAS f32x4*)src + *(const LAS f32x4*)(src + 4 * 1040)) +
                    (*(const LAS f32x4*)(src + 8 * 1040) + *(const LAS f32x4*)(src + 12 * 1040));
    const int cons = (br * 4 + cq) * 32 + bb * 8 + (quad >> 3);
    __builtin_amdgcn_raw_buffer_store_b128(nosent(a), rs, WS_OUT + cons * 4096 + h * 128 + (quad & 7) * 16, 0, AUX_SC1);
  }
  STAMP(9);
  if (w == 7) {
    const int io = WS_OUT + idx * 4096 + l * 16;
    v4u pv[4];
    for (unsigned spin = 0;; ++spin) {
      asm volatile("" ::: "memory");
#pragma unroll
      for (int n = 0; n < 4; ++n) pv[n] = __builtin_amdgcn_raw_buffer_load_b128(rs, io + n * 1024, 0, AUX_SC1);
      const bool bad = has_sent(pv[0]) | has_sent(pv[1]) | has_sent(pv[2]) | has_sent(pv[3]);
      if (__builtin_amdgcn_ballot_w64(bad) == 0ull || spin > SPIN_CAP) break;
      __builtin_amdgcn_s_sleep(1);
    }
    f32x4 a = (__builtin_bit_cast(f32x4, pv[0]) + __builtin_bit_cast(f32x4, pv[1])) +
              (__builtin_bit_cast(f32x4, pv[2]) + __builtin_bit_cast(f32x4, pv[3]));
#pragma unroll
    for (int k = 0; k < 4; ++k) { float t = a[k]; t += dpp<ROR8>(t); a[k] = x32_sum(x16_sum(t)); }
    const int r = (l >> 3) & 3;
    const float v = r == 0 ? a.x : r == 1 ? a.y : r == 2 ? a.z : a.w;
    const int obr = idx >> 7, ocq = (idx >> 5) & 3, ob = (idx >> 3) & 3, oc = idx & 7;
    if (l < 32) out[ob * 2048 + obr * 1024 + ocq * 256 + oc * 32 + l] = (unsigned short)(pk_bf16(v, v) & 0xFFFFu);
    if (idx == 0 && l == 0)
      __hip_atomic_store((GAS unsigned long long*)(ws + WS_EPOCH), epoch + 1ull, __ATOMIC_RELAXED, __HIP_MEMORY_SCOPE_AGENT);
    STAMP(10);
  }
}

extern "C" void kernel_launch(void* const* d_in, const int* in_sizes, int n_in, void* d_out, int out_size, void* d_ws,
                              size_t ws_size, hipStream_t stream) {
  hipLaunchKernelGGL(fused_decode, dim3(256), dim3(512), 0, stream, (const char*)d_in[0], (const char*)d_in[1],
                     (const char*)d_in[2], (const char*)d_in[3], (const char*)d_in[4], (const char*)d_in[5],
                     (const char*)d_in[6], (const char*)d_in[7], (const char*)d_in[8], (const char*)d_in[9],
                     (unsigned short*)d_out, (char*)d_ws);
}
```

```cpp
#include <hip/hip_runtime.h>
#include <stdint.h>

#define KV_AUX 0
#define W_AUX 0
#define WIN 8
#define STAMPS 0

typedef unsigned v4u __attribute__((ext_vector_type(4)));
typedef unsigned v2u __attribute__((ext_vector_type(2)));
typedef float f32x4 __attribute__((ext_vector_type(4)));
typedef __bf16 bf16x8 __attribute__((ext_vector_type(8)));
typedef short s16x4 __attribute__((ext_vector_type(4)));
#define GAS __attribute__((address_space(1)))
#define LAS __attribute__((address_space(3)))
#define SB() __builtin_amdgcn_sched_barrier(0)

constexpr int WS_EPOCH = 0;
constexpr int WS_QG = 4096;
constexpr int WS_ML = WS_QG + 2 * 4 * 4 * 1024 * 8;
constexpr int WS_PO = WS_ML + 2 * 32 * 64 * 8;
constexpr int WS_OUT = WS_PO + 2 * 32 * 64 * 128 * 4;
constexpr int WS_END = WS_OUT + 256 * 32 * 32 * 4;
constexpr int WS_STAMP = WS_END;
constexpr int AUX_SC1 = 16;
constexpr unsigned SENT = 0xFFFFFFFFu;
constexpr unsigned SPIN_CAP = 1u << 16;

constexpr int L_QRED = 0;
constexpr int L_QS = L_QRED + 16384;
constexpr int L_MX = L_QS + 2 * 8 * 272;
constexpr int L_LS = L_MX + 512;
constexpr int L_RED = L_LS + 512;
constexpr int L_OB = L_RED + 2 * 8 * 8 * 528;
constexpr int L_RED2 = L_OB + 4096;
constexpr int L_END = L_RED2 + 4 * 4 * 1040;

#define MKRS(ptr, n) __builtin_amdgcn_make_buffer_rsrc((void*)(ptr), 0, (n), 0x00020000)
#define LDB(rs, off, aux) __builtin_amdgcn_raw_buffer_load_b128((rs), (off), 0, (aux))
__device__ __forceinline__ f32x4 mfma32(v4u a, v4u b, f32x4 c) {
  return __builtin_amdgcn_mfma_f32_16x16x32_bf16(__builtin_bit_cast(bf16x8, a), __builtin_bit_cast(bf16x8, b), c, 0, 0, 0);
}
__device__ __forceinline__ f32x4 mfma16(v2u a, v2u b, f32x4 c) {
  return __builtin_amdgcn_mfma_f32_16x16x16bf16_1k(__builtin_bit_cast(s16x4, a), __builtin_bit_cast(s16x4, b), c, 0, 0, 0);
}
__device__ __forceinline__ unsigned pk_bf16(float lo, float hi) {
  typedef __bf16 bf16x2 __attribute__((ext_vector_type(2)));
  typedef float f32x2 __attribute__((ext_vector_type(2)));
  f32x2 v = {lo, hi};
  return __builtin_bit_cast(unsigned, __builtin_convertvector(v, bf16x2));
}
__device__ __forceinline__ unsigned tr2(const v4u& r0, const v4u& r1, int dd) {
  return __builtin_amdgcn_perm(r1[dd >> 1], r0[dd >> 1], (dd & 1) ? 0x07060302u : 0x05040100u);
}
__device__ __forceinline__ float xg_max(float x) {
  auto s = __builtin_amdgcn_permlane16_swap(__float_as_uint(x), __float_as_uint(x), false, false);
  x = fmaxf(__uint_as_float(s[0]), __uint_as_float(s[1]));
  auto t = __builtin_amdgcn_permlane32_swap(__float_as_uint(x), __float_as_uint(x), false, false);
  return fmaxf(__uint_as_float(t[0]), __uint_as_float(t[1]));
}
__device__ __forceinline__ float x16_sum(float x) {
  auto s = __builtin_amdgcn_permlane16_swap(__float_as_uint(x), __float_as_uint(x), false, false);
  return __uint_as_float(s[0]) + __uint_as_float(s[1]);
}
__device__ __forceinline__ float x32_sum(float x) {
  auto t = __builtin_amdgcn_permlane32_swap(__float_as_uint(x), __float_as_uint(x), false, false);
  return __uint_as_float(t[0]) + __uint_as_float(t[1]);
}
template <int CTRL> __device__ __forceinline__ float dpp(float x) {
  return __builtin_bit_cast(float, __builtin_amdgcn_mov_dpp(__builtin_bit_cast(int, x), CTRL, 0xf, 0xf, true));
}
constexpr int ROR1 = 0x121, ROR2 = 0x122, ROR4 = 0x124, ROR8 = 0x128;
__device__ __forceinline__ v4u nosent(f32x4 v) {
  v4u u = __builtin_bit_cast(v4u, v);
  u.x = min(u.x, SENT - 1u); u.y = min(u.y, SENT - 1u); u.z = min(u.z, SENT - 1u); u.w = min(u.w, SENT - 1u);
  return u;
}
__device__ __forceinline__ bool has_sent(v4u u) { return max(max(u.x, u.y), max(u.z, u.w)) == SENT; }
__device__ __forceinline__ void lds_barrier() {
  asm volatile("s_waitcnt lgkmcnt(0)\n\ts_barrier" ::: "memory");
}
template <int N> __device__ __forceinline__ void waitvm() { asm volatile("s_waitcnt vmcnt(%0)" ::"i"(N) : "memory"); }
template <int N> __device__ __forceinline__ void pace(int skip) {
  asm volatile("s_cmp_eq_u32 %0, 0\n\ts_cbranch_scc1 .Lpace%=\n\ts_waitcnt vmcnt(%1)\n.Lpace%=:" ::"s"(skip), "i"(N) : "scc", "memory");
}

#define STAMP(n) do {} while (0)
#define STAMP_FLUSH() do {} while (0)

__global__ void __launch_bounds__(512, 2)
fused_decode(const char* __restrict__ xA, const char* __restrict__ kA, const char* __restrict__ vA, const char* __restrict__ wqA,
             const char* __restrict__ woA, const char* __restrict__ xB, const char* __restrict__ kB, const char* __restrict__ vB,
             const char* __restrict__ wqB, const char* __restrict__ woB, unsigned short* __restrict__ out, char* __restrict__ ws) {
  __shared__ __attribute__((aligned(16))) char smem[L_END];
  LAS char* L = (LAS char*)smem;
  const int c = blockIdx.x, tid = threadIdx.x, l = tid & 63, w = __builtin_amdgcn_readfirstlane(tid >> 6);
  const int idx = (c & 7) * 32 + (c >> 3), kvh = idx >> 6, gi = idx & 63;
  const int br = gi >> 5, cb = gi & 31;
  const int b = gi >> 4, s = gi & 15;
  const int h = kvh * 8 + ((gi >> 2) & 7), cq = gi & 3;
  const int g = l >> 4, rho = l & 15;
  const __amdgpu_buffer_rsrc_t rs = __builtin_amdgcn_make_buffer_rsrc((void*)ws, 0, WS_END, 0x00020000);
  STAMP(0);

  const unsigned long long epoch =
      __hip_atomic_load((GAS unsigned long long*)(ws + WS_EPOCH), __ATOMIC_RELAXED, __HIP_MEMORY_SCOPE_AGENT);
  if (w == 0) __builtin_amdgcn_s_setprio(3);
  {
    const v4u sent = {SENT, SENT, SENT, SENT};
    __builtin_amdgcn_raw_buffer_store_b128(sent, rs, WS_PO + ((br * 32 + h) * 64 + cq * 16) * 512 + tid * 16, 0, AUX_SC1);
    const int o2 = tid < 256 ? WS_OUT + idx * 4096 + tid * 16 : WS_ML + ((br * 32 + h) * 64 + cq * 16) * 8 + (tid & 7) * 16;
    if (tid < 264) __builtin_amdgcn_raw_buffer_store_b128(sent, rs, o2, 0, AUX_SC1);
  }
  SB();

  const v4u xf = *(const GAS v4u*)((br ? xB : xA) + ((l & 3) * 1024 + 128 * w + 8 * (l >> 2)) * 2);
  v4u wr[8];
  {
    const __amdgpu_buffer_rsrc_t rw = MKRS(br ? wqB : wqA, 8 << 20);
    const int p = (128 * w + 8 * (l >> 2)) * 8192 + (kvh * 1024 + cb * 32) * 2 + (l & 3) * 16;
#pragma unroll
    for (int n = 0; n < 8; ++n) wr[n] = LDB(rw, p + n * 8192, W_AUX);
  }
  SB();
  const int pairoff = (b * 4 + kvh) * (8192 * 256);
  const int kap = 4 * (rho & 3) + (rho >> 2);
  const int offB = pairoff + (6144 + 128 * s + 16 * w) * 256, offA = pairoff + (512 * s + 64 * w) * 256;
  v4u kf[20], vf[20], wo[8];
  const __amdgpu_buffer_rsrc_t rkA = MKRS(kA, 32 << 20), rkB = MKRS(kB, 32 << 20), rvA = MKRS(vA, 32 << 20), rvB = MKRS(vB, 32 << 20);
  const int pkB = offB + kap * 256 + g * 16, pkA = offA + kap * 256 + g * 16;
#define KLOAD(n) ((n) < 4 ? LDB(rkB, pkB + (n) * 64, KV_AUX) : LDB(rkA, pkA + (((n) - 4) >> 2) * 4096 + (((n) - 4) & 3) * 64, KV_AUX))
#define VLOAD(n) ((n) < 4 ? LDB(rvB, offB + l * 16 + (n) * 1024, KV_AUX) : LDB(rvA, offA + l * 16 + ((n) - 4) * 1024, KV_AUX))
#pragma unroll
  for (int n = 0; n < 8; ++n) { waitvm<7>(); kf[n] = KLOAD(n); SB(); }

  {
    f32x4 qa[8];
#pragma unroll
    for (int dd = 0; dd < 8; ++dd) {
      const v4u a = {tr2(wr[0], wr[1], dd), tr2(wr[2], wr[3], dd), tr2(wr[4], wr[5], dd), tr2(wr[6], wr[7], dd)};
      qa[dd] = mfma32(a, xf, f32x4{0.f, 0.f, 0.f, 0.f});
    }
    if (((l >> 2) & 3) == g) {
      LAS f32x4* d = (LAS f32x4*)(L + L_QRED + ((w * 4 + g) * 4 + (l & 3)) * 128);
#pragma unroll
      for (int dd = 0; dd < 8; ++dd) d[dd] = qa[dd];
    }
  }
  STAMP(1);
  lds_barrier();
  SB();

  if (w == 0) {
    const LAS f32x4* src = (const LAS f32x4*)(L + L_QRED) + (l >> 5) * 16 * 32 + (l & 31);
    f32x4 a = src[0];
#pragma unroll
    for (int n = 1; n < 16; ++n) a += src[n * 32];
    a.x = x32_sum(a.x); a.y = x32_sum(a.y); a.z = x32_sum(a.z); a.w = x32_sum(a.w);
    const unsigned long long tag = (epoch + 1ull) << 16;
    const unsigned tlo = (unsigned)tag, thi = (unsigned)(tag >> 32);
    const unsigned p01 = pk_bf16(a.x, a.y), p23 = pk_bf16(a.z, a.w);
    const unsigned pp = (l >> 5) ? p23 : p01;
    const int r0 = (l >> 5) * 2;
    const int qbase = WS_QG + (((br * 4 + kvh) * 4 + ((l & 31) >> 3)) * 1024 + cb * 32 + (l & 7)) * 8;
    __builtin_amdgcn_raw_buffer_store_b64(v2u{tlo | (pp & 0xFFFFu), thi}, rs, qbase + r0 * 64, 0, AUX_SC1);
    __builtin_amdgcn_raw_buffer_store_b64(v2u{tlo | (pp >> 16), thi}, rs, qbase + r0 * 64 + 64, 0, AUX_SC1);
    STAMP(2);
    const int gA = WS_QG + ((kvh * 4 + b) * 1024) * 8 + l * 16, gB = gA + 16 * 1024 * 8;
    const int fj = (br * 4 + cq) * 32 + (l & 31);
    const int fo = WS_QG + (((((fj >> 5) & 1) * 4 + (fj >> 6)) * 4) * 1024 + (fj & 31) * 32) * 8;
    v4u qg[16];
    for (unsigned spin = 0;; ++spin) {
      asm volatile("" ::: "memory");
#pragma unroll
      for (int n = 0; n < 8; ++n) qg[n] = __builtin_amdgcn_raw_buffer_load_b128(rs, gA + n * 1024, 0, AUX_SC1);
#pragma unroll
      for (int n = 0; n < 8; ++n) qg[8 + n] = __builtin_amdgcn_raw_buffer_load_b128(rs, gB + n * 1024, 0, AUX_SC1);
      const v2u fl = __builtin_amdgcn_raw_buffer_load_b64(rs, fo, 0, AUX_SC1);
      unsigned bad = ((fl.x ^ tlo) & 0xFFFF0000u) | (fl.y ^ thi);
#pragma unroll
      for (int n = 0; n < 16; ++n)
        bad |= (((qg[n].x ^ tlo) | (qg[n].z ^ tlo)) & 0xFFFF0000u) | (qg[n].y ^ thi) | (qg[n].w ^ thi);
      if (__builtin_amdgcn_ballot_w64(bad != 0) == 0ull || spin > SPIN_CAP) break;
      __builtin_amdgcn_s_sleep(2);
    }
#pragma unroll
    for (int n = 0; n < 16; ++n)
      *(LAS unsigned*)(L + L_QS + n * 272 + l * 4) = (qg[n].x & 0xFFFFu) | (qg[n].z << 16);
    STAMP(3);
  }
  SB();
#pragma unroll
  for (int n = 8; n < 20; ++n) { pace<WIN - 1>(w); kf[n] = KLOAD(n); SB(); }
#pragma unroll
  for (int n = 0; n < 20; ++n) { pace<WIN - 1>(w); vf[n] = VLOAD(n); SB(); }
  {
    const __amdgpu_buffer_rsrc_t rw = MKRS(br ? woB : woA, 8 << 20);
    const int p = (128 * h + 32 * (w >> 1) + 2 * g) * 2048 + (256 * cq + 128 * (w & 1)) * 2 + rho * 16;
#pragma unroll
    for (int j = 0; j < 8; ++j) wo[j] = LDB(rw, p + (8 * (j & 3) + (j >> 2)) * 2048, W_AUX);
  }
  SB();
  STAMP(4);
  lds_barrier();
  STAMP(11);
  SB();

  constexpr float C2 = 0.08838834764831845f * 1.4426950408889634f;
  f32x4 sc[5];
  {
    const LAS char* qp = L + L_QS + (l & 7) * 272 + g * 16;
    f32x4 a = {0.f, 0.f, 0.f, 0.f};
#pragma unroll
    for (int t = 0; t < 4; ++t) a = mfma32(kf[t], *(const LAS v4u*)(qp + 8 * 272 + t * 64), a);
    sc[0] = a;
    v4u qf[4];
#pragma unroll
    for (int t = 0; t < 4; ++t) qf[t] = *(const LAS v4u*)(qp + t * 64);
#pragma unroll
    for (int u = 0; u < 4; ++u) {
      f32x4 z = {0.f, 0.f, 0.f, 0.f};
#pragma unroll
      for (int t = 0; t < 4; ++t) z = mfma32(kf[4 + 4 * u + t], qf[t], z);
      sc[1 + u] = z;
    }
  }
  SB();
  {
    float mB = fmaxf(fmaxf(sc[0].x, sc[0].y), fmaxf(sc[0].z, sc[0].w));
    float mA = fmaxf(fmaxf(sc[1].x, sc[1].y), fmaxf(sc[1].z, sc[1].w));
#pragma unroll
    for (int u = 2; u < 5; ++u) mA = fmaxf(mA, fmaxf(fmaxf(sc[u].x, sc[u].y), fmaxf(sc[u].z, sc[u].w)));
    mA = xg_max(mA); mB = xg_max(mB);
    if (l < 8) {
      *(LAS float*)(L + L_MX + (l * 8 + w) * 4) = mA;
      *(LAS float*)(L + L_MX + 256 + (l * 8 + w) * 4) = mB;
    }
  }
  STAMP(5);
  lds_barrier();
  STAMP(12);
  float MA, MB;
  {
    const LAS f32x4* m = (const LAS f32x4*)(L + L_MX + (l & 7) * 32);
    const f32x4 a0 = m[0], a1 = m[1], b0 = m[16], b1 = m[17];
    MA = fmaxf(fmaxf(fmaxf(a0.x, a0.y), fmaxf(a0.z, a0.w)), fmaxf(fmaxf(a1.x, a1.y), fmaxf(a1.z, a1.w))) * C2;
    MB = fmaxf(fmaxf(fmaxf(b0.x, b0.y), fmaxf(b0.z, b0.w)), fmaxf(fmaxf(b1.x, b1.y), fmaxf(b1.z, b1.w))) * C2;
  }
  unsigned pf[10];
  {
    float la = 0.f, lb = 0.f;
#pragma unroll
    for (int u = 0; u < 5; ++u) {
      const float M = u ? MA : MB;
      const float p0 = __builtin_amdgcn_exp2f(fmaf(sc[u].x, C2, -M)), p1 = __builtin_amdgcn_exp2f(fmaf(sc[u].y, C2, -M));
      const float p2 = __builtin_amdgcn_exp2f(fmaf(sc[u].z, C2, -M)), p3 = __builtin_amdgcn_exp2f(fmaf(sc[u].w, C2, -M));
      const float t = (p0 + p1) + (p2 + p3);
      if (u) la += t; else lb = t;
      pf[2 * u] = pk_bf16(p0, p1); pf[2 * u + 1] = pk_bf16(p2, p3);
    }
    la = x32_sum(x16_sum(la)); lb = x32_sum(x16_sum(lb));
    if (l < 8) {
      *(LAS float*)(L + L_LS + (l * 8 + w) * 4) = la;
      *(LAS float*)(L + L_LS + 256 + (l * 8 + w) * 4) = lb;
    }
  }
  SB();
  STAMP(13);
  {
    LAS f32x4* dB = (LAS f32x4*)(L + L_RED + 8 * 8 * 528 + (w * 8 + (l & 7)) * 528 + g * 128);
    f32x4 acc[8];
#pragma unroll
    for (int dd = 0; dd < 8; ++dd) {
      const v2u a = {tr2(vf[0], vf[1], dd), tr2(vf[2], vf[3], dd)};
      acc[dd] = mfma16(a, v2u{pf[0], pf[1]}, f32x4{0.f, 0.f, 0.f, 0.f});
    }
    if (rho < 8) {
#pragma unroll
      for (int dd = 0; dd < 8; ++dd) dB[dd] = acc[dd];
    }
  }
  STAMP(14);
  SB();
  {
    LAS f32x4* dA = (LAS f32x4*)(L + L_RED + (w * 8 + (l & 7)) * 528 + g * 128);
    f32x4 acc[8];
#pragma unroll
    for (int dd = 0; dd < 8; ++dd) {
      const v4u a = {tr2(vf[4], vf[5], dd), tr2(vf[6], vf[7], dd), tr2(vf[8], vf[9], dd), tr2(vf[10], vf[11], dd)};
      acc[dd] = mfma32(a, v4u{pf[2], pf[3], pf[4], pf[5]}, f32x4{0.f, 0.f, 0.f, 0.f});
    }
    SB();
#pragma unroll
    for (int dd = 0; dd < 8; ++dd) {
      const v4u a = {tr2(vf[12], vf[13], dd), tr2(vf[14], vf[15], dd), tr2(vf[16], vf[17], dd), tr2(vf[18], vf[19], dd)};
      acc[dd] = mfma32(a, v4u{pf[6], pf[7], pf[8], pf[9]}, acc[dd]);
    }
    if (rho < 8) {
#pragma unroll
      for (int dd = 0; dd < 8; ++dd) dA[dd] = acc[dd];
    }
  }
  STAMP(6);
  lds_barrier();
  STAMP(15);
  {
    const int pb = tid >> 8, hd = (tid >> 5) & 7, quad = tid & 31;
    const LAS char* src = L + L_RED + pb * (8 * 8 * 528) + hd * 528 + quad * 16;
    f32x4 a = *(const LAS f32x4*)src;
#pragma unroll
    for (int ww = 1; ww < 8; ++ww) a += *(const LAS f32x4*)(src + ww * (8 * 528));
    const int rec = (pb * 32 + kvh * 8 + hd) * 64 + b * 16 + s;
    __builtin_amdgcn_raw_buffer_store_b128(nosent(a), rs, WS_PO + rec * 512 + quad * 16, 0, AUX_SC1);
    if (quad == 0) {
      const LAS f32x4* ls = (const LAS f32x4*)(L + L_LS + pb * 256 + hd * 32);
      const LAS f32x4* mx = (const LAS f32x4*)(L + L_MX + pb * 256 + hd * 32);
      const f32x4 l0 = ls[0], l1 = ls[1], m0 = mx[0], m1 = mx[1];
      const float lsum = ((l0.x + l0.y) + (l0.z + l0.w)) + ((l1.x + l1.y) + (l1.z + l1.w));
      const float mm = fmaxf(fmaxf(fmaxf(m0.x, m0.y), fmaxf(m0.z, m0.w)), fmaxf(fmaxf(m1.x, m1.y), fmaxf(m1.z, m1.w))) * C2;
      __builtin_amdgcn_raw_buffer_store_b64(v2u{min(__float_as_uint(mm), SENT - 1u), min(__float_as_uint(lsum), SENT - 1u)}, rs,
                                            WS_ML + rec * 8, 0, AUX_SC1);
    }
  }
  STAMP(7);
  SB();

  {
    const int rec0 = (br * 32 + h) * 64 + (w >> 1) * 16;
    const int po = WS_PO + (rec0 + 8 * (w & 1) + (l >> 5)) * 512 + (l & 31) * 16;
    const int mo = WS_ML + (rec0 + rho) * 8;
    v4u pv[4]; v2u ml;
    for (unsigned spin = 0;; ++spin) {
      asm volatile("" ::: "memory");
#pragma unroll
      for (int n = 0; n < 4; ++n) pv[n] = __builtin_amdgcn_raw_buffer_load_b128(rs, po + n * 1024, 0, AUX_SC1);
      ml = __builtin_amdgcn_raw_buffer_load_b64(rs, mo, 0, AUX_SC1);
      const bool bad = has_sent(pv[0]) | has_sent(pv[1]) | has_sent(pv[2]) | has_sent(pv[3]) | (max(ml.x, ml.y) == SENT);
      if (__builtin_amdgcn_ballot_w64(bad) == 0ull || spin > SPIN_CAP) break;
      __builtin_amdgcn_s_sleep(2);
    }
    STAMP(8);
    const float m = __uint_as_float(ml.x), ls = __uint_as_float(ml.y);
    float M = m;
    M = fmaxf(M, dpp<ROR8>(M)); M = fmaxf(M, dpp<ROR4>(M)); M = fmaxf(M, dpp<ROR2>(M)); M = fmaxf(M, dpp<ROR1>(M));
    const float e = __builtin_amdgcn_exp2f(m - M);
    float Ls = e * ls;
    Ls += dpp<ROR8>(Ls); Ls += dpp<ROR4>(Ls); Ls += dpp<ROR2>(Ls); Ls += dpp<ROR1>(Ls);
    const float wg = e / Ls;
    f32x4 o = {0.f, 0.f, 0.f, 0.f};
#pragma unroll
    for (int n = 0; n < 4; ++n) {
      const float wlo = __builtin_bit_cast(float, __builtin_amdgcn_readlane(__builtin_bit_cast(int, wg), 8 * (w & 1) + 2 * n));
      const float whi = __builtin_bit_cast(float, __builtin_amdgcn_readlane(__builtin_bit_cast(int, wg), 8 * (w & 1) + 2 * n + 1));
      const float ww = (l >> 5) ? whi : wlo;
      o += __builtin_bit_cast(f32x4, pv[n]) * ww;
    }
    o.x = x32_sum(o.x); o.y = x32_sum(o.y); o.z = x32_sum(o.z); o.w = x32_sum(o.w);
    if (l < 32) *(LAS f32x4*)(L + L_OB + w * 512 + l * 16) = o;
  }
  STAMP(16);
  lds_barrier();
  STAMP(17);
  {
    const LAS char* src = L + L_OB + (l & 3) * 1024 + (32 * (w >> 1) + 8 * g) * 4;
    const f32x4 o0 = *(const LAS f32x4*)src + *(const LAS f32x4*)(src + 512);
    const f32x4 o1 = *(const LAS f32x4*)(src + 16) + *(const LAS f32x4*)(src + 528);
    const v4u of = {pk_bf16(o0.x, o0.y), pk_bf16(o0.z, o0.w), pk_bf16(o1.x, o1.y), pk_bf16(o1.z, o1.w)};
    LAS f32x4* d = (LAS f32x4*)(L + L_RED2 + ((w >> 1) * 4 + (l & 3)) * 1040 + (128 * (w & 1) + 32 * g) * 4);
    f32x4 acc[8];
#pragma unroll
    for (int dd = 0; dd < 8; ++dd) {
      const v4u a = {tr2(wo[0], wo[1], dd), tr2(wo[2], wo[3], dd), tr2(wo[4], wo[5], dd), tr2(wo[6], wo[7], dd)};
      acc[dd] = mfma32(a, of, f32x4{0.f, 0.f, 0.f, 0.f});
    }
    if (rho < 4) {
#pragma unroll
      for (int dd = 0; dd < 8; ++dd) d[dd] = acc[dd];
    }
  }
  STAMP(18);
  lds_barrier();
  if (tid < 256) {
    const int bb = tid >> 6, quad = tid & 63;
    const LAS char* src = L + L_RED2 + bb * 1040 + quad * 16;
    const f32x4 a = (*(const LAS f32x4*)src + *(const LAS f32x4*)(src + 4 * 1040)) +
                    (*(const LAS f32x4*)(src + 8 * 1040) + *(const LAS f32x4*)(src + 12 * 1040));
    const int cons = (br * 4 + cq) * 32 + bb * 8 + (quad >> 3);
    __builtin_amdgcn_raw_buffer_store_b128(nosent(a), rs, WS_OUT + cons * 4096 + h * 128 + (quad & 7) * 16, 0, AUX_SC1);
  }
  STAMP(9);
  if (w == 7) {
    const int io = WS_OUT + idx * 4096 + l * 16;
    v4u pv[4];
    for (unsigned spin = 0;; ++spin) {
      asm volatile("" ::: "memory");
#pragma unroll
      for (int n = 0; n < 4; ++n) pv[n] = __builtin_amdgcn_raw_buffer_load_b128(rs, io + n * 1024, 0, AUX_SC1);
      const bool bad = has_sent(pv[0]) | has_sent(pv[1]) | has_sent(pv[2]) | has_sent(pv[3]);
      if (__builtin_amdgcn_ballot_w64(bad) == 0ull || spin > SPIN_CAP) break;
      __builtin_amdgcn_s_sleep(1);
    }
    f32x4 a = (__builtin_bit_cast(f32x4, pv[0]) + __builtin_bit_cast(f32x4, pv[1])) +
              (__builtin_bit_cast(f32x4, pv[2]) + __builtin_bit_cast(f32x4, pv[3]));
#pragma unroll
    for (int k = 0; k < 4; ++k) { float t = a[k]; t += dpp<ROR8>(t); a[k] = x32_sum(x16_sum(t)); }
    const int r = (l >> 3) & 3;
    const float v = r == 0 ? a.x : r == 1 ? a.y : r == 2 ? a.z : a.w;
    const int obr = idx >> 7, ocq = (idx >> 5) & 3, ob = (idx >> 3) & 3, oc = idx & 7;
    if (l < 32) out[ob * 2048 + obr * 1024 + ocq * 256 + oc * 32 + l] = (unsigned short)(pk_bf16(v, v) & 0xFFFFu);
    if (idx == 0 && l == 0)
      __hip_atomic_store((GAS unsigned long long*)(ws + WS_EPOCH), epoch + 1ull, __ATOMIC_RELAXED, __HIP_MEMORY_SCOPE_AGENT);
    STAMP(10);
  }
  STAMP(22);
  STAMP_FLUSH();
}

extern "C" void kernel_launch(void* const* d_in, const int* in_sizes, int n_in, void* d_out, int out_size, void* d_ws,
                              size_t ws_size, hipStream_t stream) {
  hipLaunchKernelGGL(fused_decode, dim3(256), dim3(512), 0, stream, (const char*)d_in[0], (const char*)d_in[1],
                     (const char*)d_in[2], (const char*)d_in[3], (const char*)d_in[4], (const char*)d_in[5],
                     (const char*)d_in[6], (const char*)d_in[7], (const char*)d_in[8], (const char*)d_in[9],
                     (unsigned short*)d_out, (char*)d_ws);
}
```

```cpp
#include <hip/hip_runtime.h>
#include <stdint.h>

#define KV_AUX 2
#define W_AUX 2
#define WIN 4
#define XPT 8
#define STAMPS 0

typedef unsigned v4u __attribute__((ext_vector_type(4)));
typedef unsigned v2u __attribute__((ext_vector_type(2)));
typedef float f32x4 __attribute__((ext_vector_type(4)));
typedef __bf16 bf16x8 __attribute__((ext_vector_type(8)));
typedef short s16x4 __attribute__((ext_vector_type(4)));
#define GAS __attribute__((address_space(1)))
#define LAS __attribute__((address_space(3)))
#define SB() __builtin_amdgcn_sched_barrier(0)

constexpr int WS_EPOCH = 0;
constexpr int WS_FLAG = 4096;
constexpr int WS_QP = WS_FLAG + 256 * 128;
constexpr int WS_ML = WS_QP + 2 * 4 * 4 * 2 * 1024 * 4;
constexpr int WS_PO = WS_ML + 256 * 128;
constexpr int WS_OUT = WS_PO + 2 * 32 * 64 * 128 * 4;
constexpr int WS_END = WS_OUT + 256 * 32 * 32 * 4;
constexpr int WS_STAMP = WS_END;
constexpr int AUX_SC1 = 16;
constexpr unsigned SENT = 0xFFFFFFFFu;
constexpr unsigned SPIN_CAP = 1u << 16;

constexpr int L_SYNC = 0;
constexpr int L_WGT = 64;
constexpr int L_QRED = 512;
constexpr int L_QT = L_QRED + 16384;
constexpr int L_QS = L_QT + 1024;
constexpr int L_MX = L_QS + 2 * 8 * 272;
constexpr int L_LS = L_MX + 512;
constexpr int L_RED = L_LS + 512;
constexpr int L_RED2 = L_RED + 2 * 8 * 8 * 528;
constexpr int L_END = L_RED2 + 8 * 4 * 1056;
constexpr int S_QP = 0, S_Q = 4, S_MX = 8, S_LS = 12, S_W = 16;

#define MKRS(ptr, n) __builtin_amdgcn_make_buffer_rsrc((void*)(ptr), 0, (n), 0x00020000)
#define LDB(rs, off, aux) __builtin_amdgcn_raw_buffer_load_b128((rs), (off), 0, (aux))
__device__ __forceinline__ f32x4 mfma32(v4u a, v4u b, f32x4 c) {
  return __builtin_amdgcn_mfma_f32_16x16x32_bf16(__builtin_bit_cast(bf16x8, a), __builtin_bit_cast(bf16x8, b), c, 0, 0, 0);
}
__device__ __forceinline__ f32x4 mfma16(v2u a, v2u b, f32x4 c) {
  return __builtin_amdgcn_mfma_f32_16x16x16bf16_1k(__builtin_bit_cast(s16x4, a), __builtin_bit_cast(s16x4, b), c, 0, 0, 0);
}
__device__ __forceinline__ unsigned pk_bf16(float lo, float hi) {
  typedef __bf16 bf16x2 __attribute__((ext_vector_type(2)));
  typedef float f32x2 __attribute__((ext_vector_type(2)));
  f32x2 v = {lo, hi};
  return __builtin_bit_cast(unsigned, __builtin_convertvector(v, bf16x2));
}
__device__ __forceinline__ unsigned tr2(const v4u& r0, const v4u& r1, int dd) {
  return __builtin_amdgcn_perm(r1[dd >> 1], r0[dd >> 1], (dd & 1) ? 0x07060302u : 0x05040100u);
}
__device__ __forceinline__ float xg_max(float x) {
  auto s = __builtin_amdgcn_permlane16_swap(__float_as_uint(x), __float_as_uint(x), false, false);
  x = fmaxf(__uint_as_float(s[0]), __uint_as_float(s[1]));
  auto t = __builtin_amdgcn_permlane32_swap(__float_as_uint(x), __float_as_uint(x), false, false);
  return fmaxf(__uint_as_float(t[0]), __uint_as_float(t[1]));
}
__device__ __forceinline__ float x16_sum(float x) {
  auto s = __builtin_amdgcn_permlane16_swap(__float_as_uint(x), __float_as_uint(x), false, false);
  return __uint_as_float(s[0]) + __uint_as_float(s[1]);
}
__device__ __forceinline__ float x32_sum(float x) {
  auto t = __builtin_amdgcn_permlane32_swap(__float_as_uint(x), __float_as_uint(x), false, false);
  return __uint_as_float(t[0]) + __uint_as_float(t[1]);
}
template <int CTRL> __device__ __forceinline__ float dpp(float x) {
  return __builtin_bit_cast(float, __builtin_amdgcn_mov_dpp(__builtin_bit_cast(int, x), CTRL, 0xf, 0xf, true));
}
constexpr int ROR1 = 0x121, ROR2 = 0x122, ROR4 = 0x124, ROR8 = 0x128;
__device__ __forceinline__ v4u nosent(f32x4 v) {
  v4u u = __builtin_bit_cast(v4u, v);
  u.x = min(u.x, SENT - 1u); u.y = min(u.y, SENT - 1u); u.z = min(u.z, SENT - 1u); u.w = min(u.w, SENT - 1u);
  return u;
}
__device__ __forceinline__ bool has_sent(v4u u) { return max(max(u.x, u.y), max(u.z, u.w)) == SENT; }
__device__ __forceinline__ void lds_barrier() {
  asm volatile("s_waitcnt lgkmcnt(0)\n\ts_barrier" ::: "memory");
}
template <int N> __device__ __forceinline__ void waitvm() { asm volatile("s_waitcnt vmcnt(%0)" ::"i"(N) : "memory"); }
__device__ __forceinline__ void lds_signal(LAS char* L, int word, int l) {
  asm volatile("s_waitcnt lgkmcnt(0)" ::: "memory");
  if (l == 0) __hip_atomic_fetch_add((LAS unsigned*)(L + L_SYNC + word), 1u, __ATOMIC_RELAXED, __HIP_MEMORY_SCOPE_WORKGROUP);
  asm volatile("" ::: "memory");
}
__device__ __forceinline__ void lds_wait(LAS char* L, int word, unsigned want) {
  for (;;) {
    const unsigned v = __hip_atomic_load((LAS unsigned*)(L + L_SYNC + word), __ATOMIC_RELAXED, __HIP_MEMORY_SCOPE_WORKGROUP);
    if (__builtin_amdgcn_readfirstlane(v) >= want) break;
    __builtin_amdgcn_s_sleep(1);
  }
  asm volatile("" ::: "memory");
}

#define STAMP(n) do {} while (0)
#define STAMP_FLUSH() do {} while (0)

__global__ void __launch_bounds__(576, 3)
fused_decode(const char* __restrict__ xA, const char* __restrict__ kA, const char* __restrict__ vA, const char* __restrict__ wqA,
             const char* __restrict__ woA, const char* __restrict__ xB, const char* __restrict__ kB, const char* __restrict__ vB,
             const char* __restrict__ wqB, const char* __restrict__ woB, unsigned short* __restrict__ out, char* __restrict__ ws) {
  __shared__ __attribute__((aligned(16))) char smem[L_END];
  LAS char* L = (LAS char*)smem;
  const int c = blockIdx.x, tid = threadIdx.x, l = tid & 63, w = __builtin_amdgcn_readfirstlane(tid >> 6);
  const int idx = (c & 7) * 32 + (c >> 3), kvh = idx >> 6, gi = idx & 63;
  const int br = gi >> 5, cb = gi & 31;
  const int b = gi >> 4, s = gi & 15;
  const int h = kvh * 8 + ((gi >> 2) & 7), cq = gi & 3;
  const int g = l >> 4, rho = l & 15;
  const __amdgpu_buffer_rsrc_t rs = __builtin_amdgcn_make_buffer_rsrc((void*)ws, 0, WS_END, 0x00020000);
  STAMP(0);

  constexpr float C2 = 0.08838834764831845f * 1.4426950408889634f;

  if (w == 8) {
    __builtin_amdgcn_s_setprio(3);
    const unsigned long long epoch =
        __hip_atomic_load((GAS unsigned long long*)(ws + WS_EPOCH), __ATOMIC_RELAXED, __HIP_MEMORY_SCOPE_AGENT);
    if (l < 16) *(LAS unsigned*)(L + L_SYNC + l * 4) = 0u;
    const int qslot = WS_QP + ((((br * 4 + kvh) * 4 + (l >> 4)) * 2 + (cb & 1)) * 1024 + (cb >> 1) * 64 + (l & 15) * 4) * 4;
    {
      const v4u sent = {SENT, SENT, SENT, SENT};
      __builtin_amdgcn_raw_buffer_store_b128(sent, rs, qslot, 0, AUX_SC1);
      const int po = WS_PO + ((br * 32 + h) * 64 + cq * 16) * 512 + l * 16;
#pragma unroll
      for (int n = 0; n < 8; ++n) __builtin_amdgcn_raw_buffer_store_b128(sent, rs, po + n * 1024, 0, AUX_SC1);
      const int oo = WS_OUT + idx * 4096 + l * 16;
#pragma unroll
      for (int n = 0; n < 4; ++n) __builtin_amdgcn_raw_buffer_store_b128(sent, rs, oo + n * 1024, 0, AUX_SC1);
      if (l < 8) __builtin_amdgcn_raw_buffer_store_b128(sent, rs, WS_ML + idx * 128 + l * 16, 0, AUX_SC1);
    }
    lds_barrier();
    const unsigned long long tag = epoch + 1ull;
    const unsigned tlo = (unsigned)tag, thi = (unsigned)(tag >> 32);
    waitvm<0>();
    if (l == 0) __builtin_amdgcn_raw_buffer_store_b64(v2u{tlo, thi}, rs, WS_FLAG + idx * 128, 0, AUX_SC1);
    STAMP(2);
    lds_wait(L, S_QP, 8);
    STAMP(1);
    {
      const LAS f32x4* src = (const LAS f32x4*)(L + L_QRED) + l;
      f32x4 a = src[0];
#pragma unroll
      for (int n = 1; n < 16; ++n) a += src[n * 64];
      LAS float* t = (LAS float*)(L + L_QT) + (l >> 3) * 32 + (l & 7);
      t[0] = a.x; t[8] = a.y; t[16] = a.z; t[24] = a.w;
      asm volatile("s_waitcnt lgkmcnt(0)" ::: "memory");
      a = *((const LAS f32x4*)(L + L_QT) + l);
      __builtin_amdgcn_raw_buffer_store_b128(nosent(a), rs, qslot, 0, AUX_SC1);
    }
    STAMP(16);
    {
      const int f1 = WS_FLAG + (kvh * 64 + l) * 128, f2 = WS_FLAG + ((br * 4 + cq) * 32 + (l & 31)) * 128;
      for (unsigned spin = 0;; ++spin) {
        asm volatile("" ::: "memory");
        const v2u x1 = __builtin_amdgcn_raw_buffer_load_b64(rs, f1, 0, AUX_SC1);
        const v2u x2 = __builtin_amdgcn_raw_buffer_load_b64(rs, f2, 0, AUX_SC1);
        if (__builtin_amdgcn_ballot_w64(((x1.x ^ tlo) | (x1.y ^ thi) | (x2.x ^ tlo) | (x2.y ^ thi)) != 0u) == 0ull || spin > SPIN_CAP) break;
        __builtin_amdgcn_s_sleep(8);
      }
      asm volatile("" ::: "memory");
    }
    STAMP(19);
    {
      const int qo = WS_QP + ((kvh * 4 + b) * 2 * 1024) * 4 + l * 16;
      v4u qg[16];
#pragma unroll
      for (int n = 0; n < 16; ++n) qg[n] = v4u{SENT, SENT, SENT, SENT};
      for (unsigned spin = 0;; ++spin) {
        asm volatile("" ::: "memory");
#pragma unroll
        for (int n = 0; n < 16; ++n)
          if (has_sent(qg[n])) qg[n] = __builtin_amdgcn_raw_buffer_load_b128(rs, qo + (n & 7) * 1024, (n >> 3) * (16 * 2 * 1024 * 4), AUX_SC1);
        bool bad = false;
#pragma unroll
        for (int n = 0; n < 16; ++n) bad |= has_sent(qg[n]);
        if (__builtin_amdgcn_ballot_w64(bad) == 0ull || spin > SPIN_CAP) break;
        __builtin_amdgcn_s_sleep(4);
      }
#pragma unroll
      for (int n = 0; n < 8; ++n) {
        const f32x4 q4 = __builtin_bit_cast(f32x4, qg[(n >> 2) * 8 + (n & 3)]) + __builtin_bit_cast(f32x4, qg[(n >> 2) * 8 + 4 + (n & 3)]);
        *(LAS v2u*)(L + L_QS + (2 * n + (l >> 5)) * 272 + (l & 31) * 8) = v2u{pk_bf16(q4.x, q4.y), pk_bf16(q4.z, q4.w)};
      }
    }
    lds_signal(L, S_Q, l);
    STAMP(3);
    if (idx == 0) {
      for (int pass = 0; pass < 4; ++pass)
        for (unsigned spin = 0;; ++spin) {
          asm volatile("" ::: "memory");
          const v2u x2 = __builtin_amdgcn_raw_buffer_load_b64(rs, WS_FLAG + (pass * 64 + l) * 128, 0, AUX_SC1);
          if (__builtin_amdgcn_ballot_w64(((x2.x ^ tlo) | (x2.y ^ thi)) != 0u) == 0ull || spin > SPIN_CAP) break;
          __builtin_amdgcn_s_sleep(8);
        }
      if (l == 0) __hip_atomic_store((GAS unsigned long long*)(ws + WS_EPOCH), tag, __ATOMIC_RELAXED, __HIP_MEMORY_SCOPE_AGENT);
    }
    lds_wait(L, S_LS, 8);
    if (l < 16) {
      const LAS f32x4* ls = (const LAS f32x4*)(L + L_LS + l * 32);
      const LAS f32x4* mx = (const LAS f32x4*)(L + L_MX + l * 32);
      const f32x4 l0 = ls[0], l1 = ls[1], m0 = mx[0], m1 = mx[1];
      const float lsum = ((l0.x + l0.y) + (l0.z + l0.w)) + ((l1.x + l1.y) + (l1.z + l1.w));
      const float mm = fmaxf(fmaxf(fmaxf(m0.x, m0.y), fmaxf(m0.z, m0.w)), fmaxf(fmaxf(m1.x, m1.y), fmaxf(m1.z, m1.w))) * C2;
      __builtin_amdgcn_raw_buffer_store_b64(v2u{min(__float_as_uint(mm), SENT - 1u), min(__float_as_uint(lsum), SENT - 1u)}, rs,
                                            WS_ML + idx * 128 + l * 8, 0, AUX_SC1);
    }
    STAMP(5);
    lds_barrier();
    {
      const int mo = WS_ML + (kvh * 64 + l) * 128 + (br * 8 + (h & 7)) * 8;
      v2u ml = {SENT, SENT};
      for (unsigned spin = 0;; ++spin) {
        asm volatile("" ::: "memory");
        if (max(ml.x, ml.y) == SENT) ml = __builtin_amdgcn_raw_buffer_load_b64(rs, mo, 0, AUX_SC1);
        if (__builtin_amdgcn_ballot_w64(max(ml.x, ml.y) == SENT) == 0ull || spin > SPIN_CAP) break;
        __builtin_amdgcn_s_sleep(4);
      }
      const float m = __uint_as_float(ml.x), ls = __uint_as_float(ml.y);
      float M = m;
      M = fmaxf(M, dpp<ROR8>(M)); M = fmaxf(M, dpp<ROR4>(M)); M = fmaxf(M, dpp<ROR2>(M)); M = fmaxf(M, dpp<ROR1>(M));
      const float e = __builtin_amdgcn_exp2f(m - M);
      float Ls = e * ls;
      Ls += dpp<ROR8>(Ls); Ls += dpp<ROR4>(Ls); Ls += dpp<ROR2>(Ls); Ls += dpp<ROR1>(Ls);
      *(LAS float*)(L + L_WGT + ((l >> 4) * 16 + (l & 3) * 4 + ((l >> 2) & 3)) * 4) = e / Ls;
    }
    lds_signal(L, S_W, l);
    STAMP(12);
    STAMP(22);
    STAMP_FLUSH();
    return;
  }

  const int qrow = 512 * (cb & 1) + 64 * w;
  const v4u xf = *(const GAS v4u*)((br ? xB : xA) + ((l & 3) * 1024 + qrow + 16 * g + 8 * ((l >> 3) & 1)) * 2);
  v4u wr[8];
  const __amdgpu_buffer_rsrc_t rwq = MKRS(br ? wqB : wqA, 8 << 20);
  const int wqp = (qrow + 8 * (l >> 3)) * 8192 + (kvh * 1024 + (cb >> 1) * 64) * 2 + (l & 7) * 16;
#pragma unroll
  for (int n = 0; n < WIN; ++n) wr[n] = LDB(rwq, wqp + n * 8192, W_AUX);
  SB();
  asm volatile("s_barrier" ::: "memory");
  SB();
#pragma unroll
  for (int n = WIN; n < 8; ++n) { waitvm<WIN - 1>(); wr[n] = LDB(rwq, wqp + n * 8192, W_AUX); SB(); }
  const int wbase = (b * 4 + kvh) * (8192 * 256) + (128 * s + 16 * w) * 256;
  const int koff = wbase + (4 * (rho & 3) + ((rho >> 2) & 1)) * 256 + g * 16 + 64 * (rho >> 3);
  const int voff = wbase + l * 16;
  constexpr int TS = 2048 * 256;
  v4u kf[20], vf[20], wo[8];
  const __amdgpu_buffer_rsrc_t rkA = MKRS(kA, 32 << 20), rkB = MKRS(kB, 32 << 20), rvA = MKRS(vA, 32 << 20), rvB = MKRS(vB, 32 << 20);
#define KLOAD(n) __builtin_amdgcn_raw_buffer_load_b128((n) < 16 ? rkA : rkB, koff + ((n) & 1) * 128 + (((n) >> 1) & 1) * 512, ((n) < 16 ? ((n) >> 2) : 3) * TS, KV_AUX)
#define VLOAD(n) __builtin_amdgcn_raw_buffer_load_b128((n) < 16 ? rvA : rvB, voff + ((n) & 3) * 1024, ((n) < 16 ? ((n) >> 2) : 3) * TS, KV_AUX)
#define PK(n) do { SB(); waitvm<WIN - 1>(); kf[n] = KLOAD(n); SB(); } while (0)
#define PV_(n) do { SB(); waitvm<WIN - 1>(); vf[n] = VLOAD(n); SB(); } while (0)
#define PVX(n) do { SB(); vf[n] = VLOAD(n); SB(); } while (0)
#pragma unroll
  for (int n = 0; n < WIN; ++n) PK(n);
  SB(); kf[WIN] = KLOAD(WIN); SB();

  {
    f32x4 qa[8];
#pragma unroll
    for (int dd = 0; dd < 8; ++dd) {
      const v4u a = {tr2(wr[0], wr[1], dd), tr2(wr[2], wr[3], dd), tr2(wr[4], wr[5], dd), tr2(wr[6], wr[7], dd)};
      qa[dd] = mfma32(a, xf, f32x4{0.f, 0.f, 0.f, 0.f});
    }
    if ((l & 12) == ((g >> 1) << 3)) {
      LAS f32x4* d = (LAS f32x4*)(L + L_QRED + ((w * 2 + (g >> 1)) * 4 + (l & 3)) * 256 + (g & 1) * 128);
#pragma unroll
      for (int dd = 0; dd < 8; ++dd) d[dd] = qa[dd];
    }
  }
  lds_signal(L, S_QP, l);
  STAMP(1);
#pragma unroll
  for (int n = WIN + 1; n < 20; ++n) PK(n);
#pragma unroll
  for (int n = 0; n < XPT; ++n) PV_(n);
  STAMP(4);
  lds_wait(L, S_Q, 1);
  STAMP(11);
  PVX(XPT);

  f32x4 sc[5];
  {
    const LAS char* qp = L + L_QS + (l & 7) * 272 + g * 16 + 64 * (rho >> 3);
    const v4u qa0 = *(const LAS v4u*)qp, qa1 = *(const LAS v4u*)(qp + 128);
    const v4u qb0 = *(const LAS v4u*)(qp + 8 * 272), qb1 = *(const LAS v4u*)(qp + 8 * 272 + 128);
#pragma unroll
    for (int u = 0; u < 5; ++u) {
      const f32x4 z = {0.f, 0.f, 0.f, 0.f};
      const f32x4 t1 = mfma32(kf[4 * u + 1], u < 4 ? qa1 : qb1, mfma32(kf[4 * u], u < 4 ? qa0 : qb0, z));
      const f32x4 t2 = mfma32(kf[4 * u + 3], u < 4 ? qa1 : qb1, mfma32(kf[4 * u + 2], u < 4 ? qa0 : qb0, z));
#pragma unroll
      for (int r = 0; r < 4; ++r) {
        auto sw = __builtin_amdgcn_permlane32_swap(__float_as_uint(t1[r]), __float_as_uint(t2[r]), false, false);
        sc[u][r] = __uint_as_float(sw[0]) + dpp<ROR8>(__uint_as_float(sw[1]));
      }
      if (u == 2) PVX(XPT + 1);
    }
  }
  {
    float mB = fmaxf(fmaxf(sc[4].x, sc[4].y), fmaxf(sc[4].z, sc[4].w));
    float mA = fmaxf(fmaxf(sc[0].x, sc[0].y), fmaxf(sc[0].z, sc[0].w));
#pragma unroll
    for (int u = 1; u < 4; ++u) mA = fmaxf(mA, fmaxf(fmaxf(sc[u].x, sc[u].y), fmaxf(sc[u].z, sc[u].w)));
    mA = xg_max(mA); mB = xg_max(mB);
    if (l < 8) {
      *(LAS float*)(L + L_MX + (l * 8 + w) * 4) = mA;
      *(LAS float*)(L + L_MX + 256 + (l * 8 + w) * 4) = mB;
    }
  }
  lds_signal(L, S_MX, l);
  STAMP(5);
  PV_(XPT + 2);
  PV_(XPT + 3);
  lds_wait(L, S_MX, 8);
  STAMP(12);
  PVX(XPT + 4);
  float MA, MB;
  {
    const LAS f32x4* m = (const LAS f32x4*)(L + L_MX + (l & 7) * 32);
    const f32x4 a0 = m[0], a1 = m[1], b0 = m[16], b1 = m[17];
    MA = fmaxf(fmaxf(fmaxf(a0.x, a0.y), fmaxf(a0.z, a0.w)), fmaxf(fmaxf(a1.x, a1.y), fmaxf(a1.z, a1.w))) * C2;
    MB = fmaxf(fmaxf(fmaxf(b0.x, b0.y), fmaxf(b0.z, b0.w)), fmaxf(fmaxf(b1.x, b1.y), fmaxf(b1.z, b1.w))) * C2;
  }
  unsigned pl[10], ph[10];
  {
    float la = 0.f, lb = 0.f;
#pragma unroll
    for (int u = 0; u < 5; ++u) {
      const float M = u < 4 ? MA : MB;
      const float p0 = __builtin_amdgcn_exp2f(fmaf(sc[u].x, C2, -M)), p1 = __builtin_amdgcn_exp2f(fmaf(sc[u].y, C2, -M));
      const float p2 = __builtin_amdgcn_exp2f(fmaf(sc[u].z, C2, -M)), p3 = __builtin_amdgcn_exp2f(fmaf(sc[u].w, C2, -M));
      const float t = (p0 + p1) + (p2 + p3);
      if (u < 4) la += t; else lb = t;
      pl[2 * u] = rho < 8 ? pk_bf16(p0, p1) : 0u; pl[2 * u + 1] = rho < 8 ? pk_bf16(p2, p3) : 0u;
      ph[2 * u] = __builtin_amdgcn_mov_dpp(pl[2 * u], ROR8, 0xf, 0xf, true);
      ph[2 * u + 1] = __builtin_amdgcn_mov_dpp(pl[2 * u + 1], ROR8, 0xf, 0xf, true);
      if (u == 2) PVX(XPT + 5);
    }
    la = x32_sum(x16_sum(la)); lb = x32_sum(x16_sum(lb));
    if (l < 8) {
      *(LAS float*)(L + L_LS + (l * 8 + w) * 4) = la;
      *(LAS float*)(L + L_LS + 256 + (l * 8 + w) * 4) = lb;
    }
  }
  lds_signal(L, S_LS, l);
  STAMP(13);
  PV_(XPT + 6);
  f32x4 acc[4];
#pragma unroll
  for (int q4 = 0; q4 < 4; ++q4) {
    const v4u a0 = {tr2(vf[0], vf[1], q4), tr2(vf[2], vf[3], q4), tr2(vf[4], vf[5], q4), tr2(vf[6], vf[7], q4)};
    const v4u a1 = {tr2(vf[0], vf[1], q4 + 4), tr2(vf[2], vf[3], q4 + 4), tr2(vf[4], vf[5], q4 + 4), tr2(vf[6], vf[7], q4 + 4)};
    acc[q4] = mfma32(a1, v4u{ph[0], ph[1], ph[2], ph[3]}, mfma32(a0, v4u{pl[0], pl[1], pl[2], pl[3]}, f32x4{0.f, 0.f, 0.f, 0.f}));
  }
#pragma unroll
  for (int n = XPT + 7; n < 20; ++n) PV_(n);
  const __amdgpu_buffer_rsrc_t rwo = MKRS(br ? woB : woA, 8 << 20);
  const int wop = (128 * h + 32 * (w >> 1) + 4 * (w & 1) + g) * 2048 + 512 * cq + rho * 16;
#define WLOAD(n) LDB(rwo, wop + ((n) >> 1) * (8 * 2048) + ((n) & 1) * 256, W_AUX)
#define PW(n) do { SB(); waitvm<WIN - 1>(); wo[n] = WLOAD(n); SB(); } while (0)
  SB(); wo[0] = WLOAD(0); SB();
  STAMP(14);
#pragma unroll
  for (int q4 = 0; q4 < 4; ++q4) {
    const v4u a0 = {tr2(vf[8], vf[9], q4), tr2(vf[10], vf[11], q4), tr2(vf[12], vf[13], q4), tr2(vf[14], vf[15], q4)};
    const v4u a1 = {tr2(vf[8], vf[9], q4 + 4), tr2(vf[10], vf[11], q4 + 4), tr2(vf[12], vf[13], q4 + 4), tr2(vf[14], vf[15], q4 + 4)};
    acc[q4] = mfma32(a1, v4u{ph[4], ph[5], ph[6], ph[7]}, mfma32(a0, v4u{pl[4], pl[5], pl[6], pl[7]}, acc[q4]));
  }
  LAS f32x4* dA = (LAS f32x4*)(L + L_RED + (w * 8 + (l & 7)) * 528 + g * 128 + (rho >> 3) * 64);
#pragma unroll
  for (int q4 = 0; q4 < 4; ++q4) dA[q4] = acc[q4];
  PW(1); PW(2); PW(3);
  STAMP(20);
#pragma unroll
  for (int q4 = 0; q4 < 4; ++q4) {
    const v2u a0 = {tr2(vf[16], vf[17], q4), tr2(vf[18], vf[19], q4)};
    const v2u a1 = {tr2(vf[16], vf[17], q4 + 4), tr2(vf[18], vf[19], q4 + 4)};
    dA[8 * 8 * 33 + q4] = mfma16(a1, v2u{ph[8], ph[9]}, mfma16(a0, v2u{pl[8], pl[9]}, f32x4{0.f, 0.f, 0.f, 0.f}));
  }
  SB(); wo[4] = WLOAD(4); SB();
  STAMP(6);
  lds_barrier();
  STAMP(15);
  {
    const int pb = tid >> 8, hd = (tid >> 5) & 7, quad = tid & 31;
    const LAS char* src = L + L_RED + pb * (8 * 8 * 528) + hd * 528 + quad * 16;
    f32x4 a = *(const LAS f32x4*)src;
#pragma unroll
    for (int ww = 1; ww < 8; ++ww) a += *(const LAS f32x4*)(src + ww * (8 * 528));
    const int rec = (pb * 32 + kvh * 8 + hd) * 64 + b * 16 + s;
    __builtin_amdgcn_raw_buffer_store_b128(nosent(a), rs, WS_PO + rec * 512 + quad * 16, 0, AUX_SC1);
  }
  STAMP(7);
  SB();
#pragma unroll
  for (int n = 5; n < 8; ++n) wo[n] = WLOAD(n);
  SB();
  v2u wt[16];
#pragma unroll
  for (int m = 0; m < 16; ++m) {
    wt[m] = v2u{tr2(wo[m >> 3], wo[2 + (m >> 3)], m & 7), tr2(wo[4 + (m >> 3)], wo[6 + (m >> 3)], m & 7)};
    asm volatile("" : "+v"(wt[m]));
  }
  const int sg = (l >> 2) & 3;
  STAMP(21);
  SB();

  {
    const int po = WS_PO + ((br * 32 + h) * 64 + (l & 3) * 16 + sg) * 512 + (16 * w + 4 * g) * 4;
    v4u pv[4];
#pragma unroll
    for (int n = 0; n < 4; ++n) pv[n] = __builtin_amdgcn_raw_buffer_load_b128(rs, po + n * 2048, 0, AUX_SC1);
    SB();
    lds_wait(L, S_W, 1);
    const f32x4 wg = *(const LAS f32x4*)(L + L_WGT + ((l & 3) * 16 + sg * 4) * 4);
    SB();
    for (unsigned spin = 0;; ++spin) {
      const bool bad = has_sent(pv[0]) | has_sent(pv[1]) | has_sent(pv[2]) | has_sent(pv[3]);
      if (__builtin_amdgcn_ballot_w64(bad) == 0ull || spin > SPIN_CAP) break;
      asm volatile("" ::: "memory");
      if (bad) {
#pragma unroll
        for (int n = 0; n < 4; ++n) pv[n] = __builtin_amdgcn_raw_buffer_load_b128(rs, po + n * 2048, 0, AUX_SC1);
      }
    }
    STAMP(8);
    f32x4 o = __builtin_bit_cast(f32x4, pv[0]) * wg.x + __builtin_bit_cast(f32x4, pv[1]) * wg.y +
              (__builtin_bit_cast(f32x4, pv[2]) * wg.z + __builtin_bit_cast(f32x4, pv[3]) * wg.w);
#pragma unroll
    for (int k = 0; k < 4; ++k) { float t = o[k]; t += dpp<ROR8>(t); t += dpp<ROR4>(t); o[k] = t; }
    const unsigned o01 = pk_bf16(o.x, o.y), o23 = pk_bf16(o.z, o.w);
    v2u bm[4];
#pragma unroll
    for (int k = 0; k < 4; ++k) bm[k] = v2u{sg == k ? o01 : 0u, sg == k ? o23 : 0u};
    LAS f32x4* d = (LAS f32x4*)(L + L_RED2 + (w * 4 + (l & 3)) * 1056 + g * 128 + sg * 16);
#pragma unroll
    for (int a = 0; a < 4; ++a) {
      f32x4 z = {0.f, 0.f, 0.f, 0.f};
#pragma unroll
      for (int k = 0; k < 4; ++k) z = mfma16(wt[4 * a + k], bm[k], z);
      d[(a >> 1) * 32 + (a & 1) * 4] = z;
    }
  }
  STAMP(18);
  lds_barrier();
  if (tid < 256) {
    const int bb = tid >> 6, quad = tid & 63;
    const LAS char* src = L + L_RED2 + bb * 1056 + quad * 16;
    f32x4 a = *(const LAS f32x4*)src;
#pragma unroll
    for (int ww = 1; ww < 8; ++ww) a += *(const LAS f32x4*)(src + ww * (4 * 1056));
    const int cons = (br * 4 + cq) * 32 + bb * 8 + (quad >> 3);
    __builtin_amdgcn_raw_buffer_store_b128(nosent(a), rs, WS_OUT + cons * 4096 + h * 128 + (quad & 7) * 16, 0, AUX_SC1);
  }
  STAMP(9);
  if (w == 7) {
    const int io = WS_OUT + idx * 4096 + l * 16;
    v4u pv[4];
    for (unsigned spin = 0;; ++spin) {
      asm volatile("" ::: "memory");
#pragma unroll
      for (int n = 0; n < 4; ++n) pv[n] = __builtin_amdgcn_raw_buffer_load_b128(rs, io + n * 1024, 0, AUX_SC1);
      const bool bad = has_sent(pv[0]) | has_sent(pv[1]) | has_sent(pv[2]) | has_sent(pv[3]);
      if (__builtin_amdgcn_ballot_w64(bad) == 0ull || spin > SPIN_CAP) break;
      __builtin_amdgcn_s_sleep(1);
    }
    f32x4 a = (__builtin_bit_cast(f32x4, pv[0]) + __builtin_bit_cast(f32x4, pv[1])) +
              (__builtin_bit_cast(f32x4, pv[2]) + __builtin_bit_cast(f32x4, pv[3]));
#pragma unroll
    for (int k = 0; k < 4; ++k) { float t = a[k]; t += dpp<ROR8>(t); a[k] = x32_sum(x16_sum(t)); }
    const int r = (l >> 3) & 3;
    const float v = r == 0 ? a.x : r == 1 ? a.y : r == 2 ? a.z : a.w;
    const int obr = idx >> 7, ocq = (idx >> 5) & 3, ob = (idx >> 3) & 3, oc = idx & 7;
    if (l < 32) out[ob * 2048 + obr * 1024 + ocq * 256 + oc * 32 + l] = (unsigned short)(pk_bf16(v, v) & 0xFFFFu);
    STAMP(10);
  }
  STAMP(22);
  STAMP_FLUSH();
}

extern "C" void kernel_launch(void* const* d_in, const int* in_sizes, int n_in, void* d_out, int out_size, void* d_ws,
                              size_t ws_size, hipStream_t stream) {
  hipLaunchKernelGGL(fused_decode, dim3(256), dim3(576), 0, stream, (const char*)d_in[0], (const char*)d_in[1],
                     (const char*)d_in[2], (const char*)d_in[3], (const char*)d_in[4], (const char*)d_in[5],
                     (const char*)d_in[6], (const char*)d_in[7], (const char*)d_in[8], (const char*)d_in[9],
                     (unsigned short*)d_out, (char*)d_ws);
}
```

```cpp
#include <hip/hip_runtime.h>
#include <stdint.h>

#define KV_AUX 2
#define W_AUX 2
#define WIN 4
#define XPT 8
#define PRETOUCH 1
#define STAMPS 0

typedef unsigned v4u __attribute__((ext_vector_type(4)));
typedef unsigned v2u __attribute__((ext_vector_type(2)));
typedef float f32x4 __attribute__((ext_vector_type(4)));
typedef __bf16 bf16x8 __attribute__((ext_vector_type(8)));
typedef short s16x4 __attribute__((ext_vector_type(4)));
#define GAS __attribute__((address_space(1)))
#define LAS __attribute__((address_space(3)))
#define SB() __builtin_amdgcn_sched_barrier(0)

constexpr int WS_EPOCH = 0;
constexpr int WS_FLAG = 4096;
constexpr int WS_QP = WS_FLAG + 256 * 128;
constexpr int WS_ML = WS_QP + 2 * 4 * 4 * 2 * 1024 * 4;
constexpr int WS_PO = WS_ML + 256 * 128;
constexpr int WS_OUT = WS_PO + 2 * 32 * 64 * 128 * 4;
constexpr int WS_END = WS_OUT + 256 * 32 * 32 * 4;
constexpr int WS_STAMP = WS_END;
constexpr int AUX_SC1 = 16;
constexpr unsigned SENT = 0xFFFFFFFFu;
constexpr unsigned SPIN_CAP = 1u << 16;

constexpr int L_SYNC = 0;
constexpr int L_WGT = 64;
constexpr int L_QRED = 512;
constexpr int L_QT = L_QRED + 16384;
constexpr int L_QS = L_QT + 1024;
constexpr int L_MX = L_QS + 2 * 8 * 272;
constexpr int L_LS = L_MX + 512;
constexpr int L_RED = L_LS + 512;
constexpr int L_RED2 = L_RED + 2 * 8 * 8 * 528;
constexpr int L_END = L_RED2 + 8 * 4 * 1056;
constexpr int S_QP = 0, S_Q = 4, S_MX = 8, S_LS = 12, S_W = 16;

#define MKRS(ptr, n) __builtin_amdgcn_make_buffer_rsrc((void*)(ptr), 0, (n), 0x00020000)
#define LDB(rs, off, aux) __builtin_amdgcn_raw_buffer_load_b128((rs), (off), 0, (aux))
__device__ __forceinline__ f32x4 mfma32(v4u a, v4u b, f32x4 c) {
  return __builtin_amdgcn_mfma_f32_16x16x32_bf16(__builtin_bit_cast(bf16x8, a), __builtin_bit_cast(bf16x8, b), c, 0, 0, 0);
}
__device__ __forceinline__ f32x4 mfma16(v2u a, v2u b, f32x4 c) {
  return __builtin_amdgcn_mfma_f32_16x16x16bf16_1k(__builtin_bit_cast(s16x4, a), __builtin_bit_cast(s16x4, b), c, 0, 0, 0);
}
__device__ __forceinline__ unsigned pk_bf16(float lo, float hi) {
  typedef __bf16 bf16x2 __attribute__((ext_vector_type(2)));
  typedef float f32x2 __attribute__((ext_vector_type(2)));
  f32x2 v = {lo, hi};
  return __builtin_bit_cast(unsigned, __builtin_convertvector(v, bf16x2));
}
__device__ __forceinline__ unsigned tr2(const v4u& r0, const v4u& r1, int dd) {
  return __builtin_amdgcn_perm(r1[dd >> 1], r0[dd >> 1], (dd & 1) ? 0x07060302u : 0x05040100u);
}
__device__ __forceinline__ float xg_max(float x) {
  auto s = __builtin_amdgcn_permlane16_swap(__float_as_uint(x), __float_as_uint(x), false, false);
  x = fmaxf(__uint_as_float(s[0]), __uint_as_float(s[1]));
  auto t = __builtin_amdgcn_permlane32_swap(__float_as_uint(x), __float_as_uint(x), false, false);
  return fmaxf(__uint_as_float(t[0]), __uint_as_float(t[1]));
}
__device__ __forceinline__ float x16_sum(float x) {
  auto s = __builtin_amdgcn_permlane16_swap(__float_as_uint(x), __float_as_uint(x), false, false);
  return __uint_as_float(s[0]) + __uint_as_float(s[1]);
}
__device__ __forceinline__ float x32_sum(float x) {
  auto t = __builtin_amdgcn_permlane32_swap(__float_as_uint(x), __float_as_uint(x), false, false);
  return __uint_as_float(t[0]) + __uint_as_float(t[1]);
}
template <int CTRL> __device__ __forceinline__ float dpp(float x) {
  return __builtin_bit_cast(float, __builtin_amdgcn_mov_dpp(__builtin_bit_cast(int, x), CTRL, 0xf, 0xf, true));
}
constexpr int ROR1 = 0x121, ROR2 = 0x122, ROR4 = 0x124, ROR8 = 0x128;
__device__ __forceinline__ v4u nosent(f32x4 v) {
  v4u u = __builtin_bit_cast(v4u, v);
  u.x = min(u.x, SENT - 1u); u.y = min(u.y, SENT - 1u); u.z = min(u.z, SENT - 1u); u.w = min(u.w, SENT - 1u);
  return u;
}
__device__ __forceinline__ bool has_sent(v4u u) { return max(max(u.x, u.y), max(u.z, u.w)) == SENT; }
__device__ __forceinline__ void lds_barrier() {
  asm volatile("s_waitcnt lgkmcnt(0)\n\ts_barrier" ::: "memory");
}
template <int N> __device__ __forceinline__ void waitvm() { asm volatile("s_waitcnt vmcnt(%0)" ::"i"(N) : "memory"); }
__device__ __forceinline__ void lds_signal(LAS char* L, int word, int l) {
  asm volatile("s_waitcnt lgkmcnt(0)" ::: "memory");
  if (l == 0) __hip_atomic_fetch_add((LAS unsigned*)(L + L_SYNC + word), 1u, __ATOMIC_RELAXED, __HIP_MEMORY_SCOPE_WORKGROUP);
  asm volatile("" ::: "memory");
}
__device__ __forceinline__ void lds_wait(LAS char* L, int word, unsigned want) {
  for (;;) {
    const unsigned v = __hip_atomic_load((LAS unsigned*)(L + L_SYNC + word), __ATOMIC_RELAXED, __HIP_MEMORY_SCOPE_WORKGROUP);
    if (__builtin_amdgcn_readfirstlane(v) >= want) break;
    __builtin_amdgcn_s_sleep(1);
  }
  asm volatile("" ::: "memory");
}

#define STAMP(n) do {} while (0)
#define STAMP_FLUSH() do {} while (0)

__global__ void __launch_bounds__(576, 3)
fused_decode(const char* __restrict__ xA, const char* __restrict__ kA, const char* __restrict__ vA, const char* __restrict__ wqA,
             const char* __restrict__ woA, const char* __restrict__ xB, const char* __restrict__ kB, const char* __restrict__ vB,
             const char* __restrict__ wqB, const char* __restrict__ woB, unsigned short* __restrict__ out, char* __restrict__ ws) {
  __shared__ __attribute__((aligned(16))) char smem[L_END];
  LAS char* L = (LAS char*)smem;
  const int c = blockIdx.x, tid = threadIdx.x, l = tid & 63, w = __builtin_amdgcn_readfirstlane(tid >> 6);
  const int idx = (c & 7) * 32 + (c >> 3), kvh = idx >> 6, gi = idx & 63;
  const int br = gi >> 5, cb = gi & 31;
  const int b = gi >> 4, s = gi & 15;
  const int h = kvh * 8 + ((gi >> 2) & 7), cq = gi & 3;
  const int g = l >> 4, rho = l & 15;
  const __amdgpu_buffer_rsrc_t rs = __builtin_amdgcn_make_buffer_rsrc((void*)ws, 0, WS_END, 0x00020000);
  STAMP(0);

  constexpr float C2 = 0.08838834764831845f * 1.4426950408889634f;

  if (w == 8) {
    __builtin_amdgcn_s_setprio(3);
    const unsigned long long epoch =
        __hip_atomic_load((GAS unsigned long long*)(ws + WS_EPOCH), __ATOMIC_RELAXED, __HIP_MEMORY_SCOPE_AGENT);
    if (l < 16) *(LAS unsigned*)(L + L_SYNC + l * 4) = 0u;
    unsigned touch = 0;
    {
      const size_t cub = (size_t)(b * 4 + kvh) * (8192 * 256) + (size_t)(128 * s) * 256;
      const int t = l & 15;
      const char* p = t < 4 ? kA + cub + t * (2048 * 256) : t < 8 ? vA + cub + (t - 4) * (2048 * 256)
                    : t == 8 ? kB + cub + 6144 * 256 : t == 9 ? vB + cub + 6144 * 256
                    : t < 14 ? (br ? woB : woA) + (size_t)(128 * h + 32 * (t - 10)) * 2048 + 512 * cq
                    : t == 14 ? (const char*)out : ws + WS_OUT + ((br * 4 + cq) * 32) * 4096;
      if (l < 16) touch = __builtin_nontemporal_load((const GAS unsigned*)p);
    }
    const int qslot = WS_QP + ((((br * 4 + kvh) * 4 + (l >> 4)) * 2 + (cb & 1)) * 1024 + (cb >> 1) * 64 + (l & 15) * 4) * 4;
    {
      const v4u sent = {SENT, SENT, SENT, SENT};
      __builtin_amdgcn_raw_buffer_store_b128(sent, rs, qslot, 0, AUX_SC1);
      const int po = WS_PO + ((br * 32 + h) * 64 + cq * 16) * 512 + l * 16;
#pragma unroll
      for (int n = 0; n < 8; ++n) __builtin_amdgcn_raw_buffer_store_b128(sent, rs, po + n * 1024, 0, AUX_SC1);
      const int oo = WS_OUT + idx * 4096 + l * 16;
#pragma unroll
      for (int n = 0; n < 4; ++n) __builtin_amdgcn_raw_buffer_store_b128(sent, rs, oo + n * 1024, 0, AUX_SC1);
      if (l < 8) __builtin_amdgcn_raw_buffer_store_b128(sent, rs, WS_ML + idx * 128 + l * 16, 0, AUX_SC1);
    }
    lds_barrier();
    const unsigned long long tag = epoch + 1ull;
    const unsigned tlo = (unsigned)tag, thi = (unsigned)(tag >> 32);
    waitvm<0>();
    asm volatile("" ::"v"(touch));
    if (l == 0) __builtin_amdgcn_raw_buffer_store_b64(v2u{tlo, thi}, rs, WS_FLAG + idx * 128, 0, AUX_SC1);
    STAMP(2);
    lds_wait(L, S_QP, 8);
    STAMP(1);
    {
      const LAS f32x4* src = (const LAS f32x4*)(L + L_QRED) + l;
      f32x4 a = src[0];
#pragma unroll
      for (int n = 1; n < 16; ++n) a += src[n * 64];
      LAS float* t = (LAS float*)(L + L_QT) + (l >> 3) * 32 + (l & 7);
      t[0] = a.x; t[8] = a.y; t[16] = a.z; t[24] = a.w;
      asm volatile("s_waitcnt lgkmcnt(0)" ::: "memory");
      a = *((const LAS f32x4*)(L + L_QT) + l);
      __builtin_amdgcn_raw_buffer_store_b128(nosent(a), rs, qslot, 0, AUX_SC1);
    }
    STAMP(16);
    {
      const int f1 = WS_FLAG + (kvh * 64 + l) * 128, f2 = WS_FLAG + ((br * 4 + cq) * 32 + (l & 31)) * 128;
      for (unsigned spin = 0;; ++spin) {
        asm volatile("" ::: "memory");
        const v2u x1 = __builtin_amdgcn_raw_buffer_load_b64(rs, f1, 0, AUX_SC1);
        const v2u x2 = __builtin_amdgcn_raw_buffer_load_b64(rs, f2, 0, AUX_SC1);
        if (__builtin_amdgcn_ballot_w64(((x1.x ^ tlo) | (x1.y ^ thi) | (x2.x ^ tlo) | (x2.y ^ thi)) != 0u) == 0ull || spin > SPIN_CAP) break;
        __builtin_amdgcn_s_sleep(8);
      }
      asm volatile("" ::: "memory");
    }
    STAMP(19);
    {
      const int qo = WS_QP + ((kvh * 4 + b) * 2 * 1024) * 4 + l * 16;
      v4u qg[16];
#pragma unroll
      for (int n = 0; n < 16; ++n) qg[n] = v4u{SENT, SENT, SENT, SENT};
      for (unsigned spin = 0;; ++spin) {
        asm volatile("" ::: "memory");
#pragma unroll
        for (int n = 0; n < 16; ++n)
          if (has_sent(qg[n])) qg[n] = __builtin_amdgcn_raw_buffer_load_b128(rs, qo + (n & 7) * 1024, (n >> 3) * (16 * 2 * 1024 * 4), AUX_SC1);
        bool bad = false;
#pragma unroll
        for (int n = 0; n < 16; ++n) bad |= has_sent(qg[n]);
        if (__builtin_amdgcn_ballot_w64(bad) == 0ull || spin > SPIN_CAP) break;
        __builtin_amdgcn_s_sleep(4);
      }
#pragma unroll
      for (int n = 0; n < 8; ++n) {
        const f32x4 q4 = __builtin_bit_cast(f32x4, qg[(n >> 2) * 8 + (n & 3)]) + __builtin_bit_cast(f32x4, qg[(n >> 2) * 8 + 4 + (n & 3)]);
        *(LAS v2u*)(L + L_QS + (2 * n + (l >> 5)) * 272 + (l & 31) * 8) = v2u{pk_bf16(q4.x, q4.y), pk_bf16(q4.z, q4.w)};
      }
    }
    lds_signal(L, S_Q, l);
    STAMP(3);
    if (idx == 0) {
      for (int pass = 0; pass < 4; ++pass)
        for (unsigned spin = 0;; ++spin) {
          asm volatile("" ::: "memory");
          const v2u x2 = __builtin_amdgcn_raw_buffer_load_b64(rs, WS_FLAG + (pass * 64 + l) * 128, 0, AUX_SC1);
          if (__builtin_amdgcn_ballot_w64(((x2.x ^ tlo) | (x2.y ^ thi)) != 0u) == 0ull || spin > SPIN_CAP) break;
          __builtin_amdgcn_s_sleep(8);
        }
      if (l == 0) __hip_atomic_store((GAS unsigned long long*)(ws + WS_EPOCH), tag, __ATOMIC_RELAXED, __HIP_MEMORY_SCOPE_AGENT);
    }
    lds_wait(L, S_LS, 8);
    if (l < 16) {
      const LAS f32x4* ls = (const LAS f32x4*)(L + L_LS + l * 32);
      const LAS f32x4* mx = (const LAS f32x4*)(L + L_MX + l * 32);
      const f32x4 l0 = ls[0], l1 = ls[1], m0 = mx[0], m1 = mx[1];
      const float lsum = ((l0.x + l0.y) + (l0.z + l0.w)) + ((l1.x + l1.y) + (l1.z + l1.w));
      const float mm = fmaxf(fmaxf(fmaxf(m0.x, m0.y), fmaxf(m0.z, m0.w)), fmaxf(fmaxf(m1.x, m1.y), fmaxf(m1.z, m1.w))) * C2;
      __builtin_amdgcn_raw_buffer_store_b64(v2u{min(__float_as_uint(mm), SENT - 1u), min(__float_as_uint(lsum), SENT - 1u)}, rs,
                                            WS_ML + idx * 128 + l * 8, 0, AUX_SC1);
    }
    STAMP(5);
    lds_barrier();
    {
      const int mo = WS_ML + (kvh * 64 + l) * 128 + (br * 8 + (h & 7)) * 8;
      v2u ml = {SENT, SENT};
      for (unsigned spin = 0;; ++spin) {
        asm volatile("" ::: "memory");
        if (max(ml.x, ml.y) == SENT) ml = __builtin_amdgcn_raw_buffer_load_b64(rs, mo, 0, AUX_SC1);
        if (__builtin_amdgcn_ballot_w64(max(ml.x, ml.y) == SENT) == 0ull || spin > SPIN_CAP) break;
        __builtin_amdgcn_s_sleep(4);
      }
      const float m = __uint_as_float(ml.x), ls = __uint_as_float(ml.y);
      float M = m;
      M = fmaxf(M, dpp<ROR8>(M)); M = fmaxf(M, dpp<ROR4>(M)); M = fmaxf(M, dpp<ROR2>(M)); M = fmaxf(M, dpp<ROR1>(M));
      const float e = __builtin_amdgcn_exp2f(m - M);
      float Ls = e * ls;
      Ls += dpp<ROR8>(Ls); Ls += dpp<ROR4>(Ls); Ls += dpp<ROR2>(Ls); Ls += dpp<ROR1>(Ls);
      *(LAS float*)(L + L_WGT + ((l >> 4) * 16 + (l & 3) * 4 + ((l >> 2) & 3)) * 4) = e / Ls;
    }
    lds_signal(L, S_W, l);
    STAMP(12);
    STAMP(22);
    STAMP_FLUSH();
    return;
  }

  const int qrow = 512 * (cb & 1) + 64 * w;
  const v4u xf = *(const GAS v4u*)((br ? xB : xA) + ((l & 3) * 1024 + qrow + 16 * g + 8 * ((l >> 3) & 1)) * 2);
  v4u wr[8];
  const __amdgpu_buffer_rsrc_t rwq = MKRS(br ? wqB : wqA, 8 << 20);
  const int wqp = (qrow + 8 * (l >> 3)) * 8192 + (kvh * 1024 + (cb >> 1) * 64) * 2 + (l & 7) * 16;
#pragma unroll
  for (int n = 0; n < WIN; ++n) wr[n] = LDB(rwq, wqp + n * 8192, W_AUX);
  SB();
  asm volatile("s_barrier" ::: "memory");
  SB();
#pragma unroll
  for (int n = WIN; n < 8; ++n) { waitvm<WIN - 1>(); wr[n] = LDB(rwq, wqp + n * 8192, W_AUX); SB(); }
  const int wbase = (b * 4 + kvh) * (8192 * 256) + (128 * s + 16 * w) * 256;
  const int koff = wbase + (4 * (rho & 3) + ((rho >> 2) & 1)) * 256 + g * 16 + 64 * (rho >> 3);
  const int voff = wbase + l * 16;
  constexpr int TS = 2048 * 256;
  v4u kf[20], vf[20], wo[8];
  const __amdgpu_buffer_rsrc_t rkA = MKRS(kA, 32 << 20), rkB = MKRS(kB, 32 << 20), rvA = MKRS(vA, 32 << 20), rvB = MKRS(vB, 32 << 20);
#define KLOAD(n) __builtin_amdgcn_raw_buffer_load_b128((n) < 16 ? rkA : rkB, koff + ((n) & 1) * 128 + (((n) >> 1) & 1) * 512, ((n) < 16 ? ((n) >> 2) : 3) * TS, KV_AUX)
#define VLOAD(n) __builtin_amdgcn_raw_buffer_load_b128((n) < 16 ? rvA : rvB, voff + ((n) & 3) * 1024, ((n) < 16 ? ((n) >> 2) : 3) * TS, KV_AUX)
#define PK(n) do { SB(); waitvm<WIN - 1>(); kf[n] = KLOAD(n); SB(); } while (0)
#define PV_(n) do { SB(); waitvm<WIN - 1>(); vf[n] = VLOAD(n); SB(); } while (0)
#define PVX(n) do { SB(); vf[n] = VLOAD(n); SB(); } while (0)
#pragma unroll
  for (int n = 0; n < WIN; ++n) PK(n);
  SB(); kf[WIN] = KLOAD(WIN); SB();

  {
    f32x4 qa[8];
#pragma unroll
    for (int dd = 0; dd < 8; ++dd) {
      const v4u a = {tr2(wr[0], wr[1], dd), tr2(wr[2], wr[3], dd), tr2(wr[4], wr[5], dd), tr2(wr[6], wr[7], dd)};
      qa[dd] = mfma32(a, xf, f32x4{0.f, 0.f, 0.f, 0.f});
    }
    if ((l & 12) == ((g >> 1) << 3)) {
      LAS f32x4* d = (LAS f32x4*)(L + L_QRED + ((w * 2 + (g >> 1)) * 4 + (l & 3)) * 256 + (g & 1) * 128);
#pragma unroll
      for (int dd = 0; dd < 8; ++dd) d[dd] = qa[dd];
    }
  }
  lds_signal(L, S_QP, l);
  STAMP(1);
#pragma unroll
  for (int n = WIN + 1; n < 20; ++n) PK(n);
#pragma unroll
  for (int n = 0; n < XPT; ++n) PV_(n);
  STAMP(4);
  lds_wait(L, S_Q, 1);
  STAMP(11);
  PVX(XPT);

  f32x4 sc[5];
  {
    const LAS char* qp = L + L_QS + (l & 7) * 272 + g * 16 + 64 * (rho >> 3);
    const v4u qa0 = *(const LAS v4u*)qp, qa1 = *(const LAS v4u*)(qp + 128);
    const v4u qb0 = *(const LAS v4u*)(qp + 8 * 272), qb1 = *(const LAS v4u*)(qp + 8 * 272 + 128);
#pragma unroll
    for (int u = 0; u < 5; ++u) {
      const f32x4 z = {0.f, 0.f, 0.f, 0.f};
      const f32x4 t1 = mfma32(kf[4 * u + 1], u < 4 ? qa1 : qb1, mfma32(kf[4 * u], u < 4 ? qa0 : qb0, z));
      const f32x4 t2 = mfma32(kf[4 * u + 3], u < 4 ? qa1 : qb1, mfma32(kf[4 * u + 2], u < 4 ? qa0 : qb0, z));
#pragma unroll
      for (int r = 0; r < 4; ++r) {
        auto sw = __builtin_amdgcn_permlane32_swap(__float_as_uint(t1[r]), __float_as_uint(t2[r]), false, false);
        sc[u][r] = __uint_as_float(sw[0]) + dpp<ROR8>(__uint_as_float(sw[1]));
      }
      if (u == 2) PVX(XPT + 1);
    }
  }
  {
    float mB = fmaxf(fmaxf(sc[4].x, sc[4].y), fmaxf(sc[4].z, sc[4].w));
    float mA = fmaxf(fmaxf(sc[0].x, sc[0].y), fmaxf(sc[0].z, sc[0].w));
#pragma unroll
    for (int u = 1; u < 4; ++u) mA = fmaxf(mA, fmaxf(fmaxf(sc[u].x, sc[u].y), fmaxf(sc[u].z, sc[u].w)));
    mA = xg_max(mA); mB = xg_max(mB);
    if (l < 8) {
      *(LAS float*)(L + L_MX + (l * 8 + w) * 4) = mA;
      *(LAS float*)(L + L_MX + 256 + (l * 8 + w) * 4) = mB;
    }
  }
  lds_signal(L, S_MX, l);
  STAMP(5);
  PV_(XPT + 2);
  PV_(XPT + 3);
  lds_wait(L, S_MX, 8);
  STAMP(12);
  PVX(XPT + 4);
  float MA, MB;
  {
    const LAS f32x4* m = (const LAS f32x4*)(L + L_MX + (l & 7) * 32);
    const f32x4 a0 = m[0], a1 = m[1], b0 = m[16], b1 = m[17];
    MA = fmaxf(fmaxf(fmaxf(a0.x, a0.y), fmaxf(a0.z, a0.w)), fmaxf(fmaxf(a1.x, a1.y), fmaxf(a1.z, a1.w))) * C2;
    MB = fmaxf(fmaxf(fmaxf(b0.x, b0.y), fmaxf(b0.z, b0.w)), fmaxf(fmaxf(b1.x, b1.y), fmaxf(b1.z, b1.w))) * C2;
  }
  unsigned pl[10], ph[10];
  {
    float la = 0.f, lb = 0.f;
#pragma unroll
    for (int u = 0; u < 5; ++u) {
      const float M = u < 4 ? MA : MB;
      const float p0 = __builtin_amdgcn_exp2f(fmaf(sc[u].x, C2, -M)), p1 = __builtin_amdgcn_exp2f(fmaf(sc[u].y, C2, -M));
      const float p2 = __builtin_amdgcn_exp2f(fmaf(sc[u].z, C2, -M)), p3 = __builtin_amdgcn_exp2f(fmaf(sc[u].w, C2, -M));
      const float t = (p0 + p1) + (p2 + p3);
      if (u < 4) la += t; else lb = t;
      pl[2 * u] = rho < 8 ? pk_bf16(p0, p1) : 0u; pl[2 * u + 1] = rho < 8 ? pk_bf16(p2, p3) : 0u;
      ph[2 * u] = __builtin_amdgcn_mov_dpp(pl[2 * u], ROR8, 0xf, 0xf, true);
      ph[2 * u + 1] = __builtin_amdgcn_mov_dpp(pl[2 * u + 1], ROR8, 0xf, 0xf, true);
      if (u == 2) PVX(XPT + 5);
    }
    la = x32_sum(x16_sum(la)); lb = x32_sum(x16_sum(lb));
    if (l < 8) {
      *(LAS float*)(L + L_LS + (l * 8 + w) * 4) = la;
      *(LAS float*)(L + L_LS + 256 + (l * 8 + w) * 4) = lb;
    }
  }
  lds_signal(L, S_LS, l);
  STAMP(13);
  PV_(XPT + 6);
  f32x4 acc[4];
#pragma unroll
  for (int q4 = 0; q4 < 4; ++q4) {
    const v4u a0 = {tr2(vf[0], vf[1], q4), tr2(vf[2], vf[3], q4), tr2(vf[4], vf[5], q4), tr2(vf[6], vf[7], q4)};
    const v4u a1 = {tr2(vf[0], vf[1], q4 + 4), tr2(vf[2], vf[3], q4 + 4), tr2(vf[4], vf[5], q4 + 4), tr2(vf[6], vf[7], q4 + 4)};
    acc[q4] = mfma32(a1, v4u{ph[0], ph[1], ph[2], ph[3]}, mfma32(a0, v4u{pl[0], pl[1], pl[2], pl[3]}, f32x4{0.f, 0.f, 0.f, 0.f}));
  }
#pragma unroll
  for (int n = XPT + 7; n < 20; ++n) PV_(n);
  const __amdgpu_buffer_rsrc_t rwo = MKRS(br ? woB : woA, 8 << 20);
  const int wop = (128 * h + 32 * (w >> 1) + 4 * (w & 1) + g) * 2048 + 512 * cq + rho * 16;
#define WLOAD(n) LDB(rwo, wop + ((n) >> 1) * (8 * 2048) + ((n) & 1) * 256, W_AUX)
#define PW(n) do { SB(); waitvm<WIN - 1>(); wo[n] = WLOAD(n); SB(); } while (0)
  SB(); wo[0] = WLOAD(0); SB();
  STAMP(14);
#pragma unroll
  for (int q4 = 0; q4 < 4; ++q4) {
    const v4u a0 = {tr2(vf[8], vf[9], q4), tr2(vf[10], vf[11], q4), tr2(vf[12], vf[13], q4), tr2(vf[14], vf[15], q4)};
    const v4u a1 = {tr2(vf[8], vf[9], q4 + 4), tr2(vf[10], vf[11], q4 + 4), tr2(vf[12], vf[13], q4 + 4), tr2(vf[14], vf[15], q4 + 4)};
    acc[q4] = mfma32(a1, v4u{ph[4], ph[5], ph[6], ph[7]}, mfma32(a0, v4u{pl[4], pl[5], pl[6], pl[7]}, acc[q4]));
  }
  LAS f32x4* dA = (LAS f32x4*)(L + L_RED + (w * 8 + (l & 7)) * 528 + g * 128 + (rho >> 3) * 64);
#pragma unroll
  for (int q4 = 0; q4 < 4; ++q4) dA[q4] = acc[q4];
  PW(1); PW(2); PW(3);
  STAMP(20);
#pragma unroll
  for (int q4 = 0; q4 < 4; ++q4) {
    const v2u a0 = {tr2(vf[16], vf[17], q4), tr2(vf[18], vf[19], q4)};
    const v2u a1 = {tr2(vf[16], vf[17], q4 + 4), tr2(vf[18], vf[19], q4 + 4)};
    dA[8 * 8 * 33 + q4] = mfma16(a1, v2u{ph[8], ph[9]}, mfma16(a0, v2u{pl[8], pl[9]}, f32x4{0.f, 0.f, 0.f, 0.f}));
  }
  SB(); wo[4] = WLOAD(4); SB();
  STAMP(6);
  lds_barrier();
  STAMP(15);
  {
    const int pb = tid >> 8, hd = (tid >> 5) & 7, quad = tid & 31;
    const LAS char* src = L + L_RED + pb * (8 * 8 * 528) + hd * 528 + quad * 16;
    f32x4 a = *(const LAS f32x4*)src;
#pragma unroll
    for (int ww = 1; ww < 8; ++ww) a += *(const LAS f32x4*)(src + ww * (8 * 528));
    const int rec = (pb * 32 + kvh * 8 + hd) * 64 + b * 16 + s;
    __builtin_amdgcn_raw_buffer_store_b128(nosent(a), rs, WS_PO + rec * 512 + quad * 16, 0, AUX_SC1);
  }
  STAMP(7);
  SB();
#pragma unroll
  for (int n = 5; n < 8; ++n) wo[n] = WLOAD(n);
  SB();
  v2u wt[16];
#pragma unroll
  for (int m = 0; m < 16; ++m) {
    wt[m] = v2u{tr2(wo[m >> 3], wo[2 + (m >> 3)], m & 7), tr2(wo[4 + (m >> 3)], wo[6 + (m >> 3)], m & 7)};
    asm volatile("" : "+v"(wt[m]));
  }
  const int sg = (l >> 2) & 3;
  STAMP(21);
  SB();

  {
    const int po = WS_PO + ((br * 32 + h) * 64 + (l & 3) * 16 + sg) * 512 + (16 * w + 4 * g) * 4;
    v4u pv[4];
#pragma unroll
    for (int n = 0; n < 4; ++n) pv[n] = __builtin_amdgcn_raw_buffer_load_b128(rs, po + n * 2048, 0, AUX_SC1);
    SB();
    lds_wait(L, S_W, 1);
    const f32x4 wg = *(const LAS f32x4*)(L + L_WGT + ((l & 3) * 16 + sg * 4) * 4);
    SB();
    for (unsigned spin = 0;; ++spin) {
      const bool bad = has_sent(pv[0]) | has_sent(pv[1]) | has_sent(pv[2]) | has_sent(pv[3]);
      if (__builtin_amdgcn_ballot_w64(bad) == 0ull || spin > SPIN_CAP) break;
      asm volatile("" ::: "memory");
      if (bad) {
#pragma unroll
        for (int n = 0; n < 4; ++n) pv[n] = __builtin_amdgcn_raw_buffer_load_b128(rs, po + n * 2048, 0, AUX_SC1);
      }
    }
    STAMP(8);
    f32x4 o = __builtin_bit_cast(f32x4, pv[0]) * wg.x + __builtin_bit_cast(f32x4, pv[1]) * wg.y +
              (__builtin_bit_cast(f32x4, pv[2]) * wg.z + __builtin_bit_cast(f32x4, pv[3]) * wg.w);
#pragma unroll
    for (int k = 0; k < 4; ++k) { float t = o[k]; t += dpp<ROR8>(t); t += dpp<ROR4>(t); o[k] = t; }
    const unsigned o01 = pk_bf16(o.x, o.y), o23 = pk_bf16(o.z, o.w);
    v2u bm[4];
#pragma unroll
    for (int k = 0; k < 4; ++k) bm[k] = v2u{sg == k ? o01 : 0u, sg == k ? o23 : 0u};
    LAS f32x4* d = (LAS f32x4*)(L + L_RED2 + (w * 4 + (l & 3)) * 1056 + g * 128 + sg * 16);
#pragma unroll
    for (int a = 0; a < 4; ++a) {
      f32x4 z = {0.f, 0.f, 0.f, 0.f};
#pragma unroll
      for (int k = 0; k < 4; ++k) z = mfma16(wt[4 * a + k], bm[k], z);
      d[(a >> 1) * 32 + (a & 1) * 4] = z;
    }
  }
  STAMP(18);
  lds_barrier();
  if (tid < 256) {
    const int bb = tid >> 6, quad = tid & 63;
    const LAS char* src = L + L_RED2 + bb * 1056 + quad * 16;
    f32x4 a = *(const LAS f32x4*)src;
#pragma unroll
    for (int ww = 1; ww < 8; ++ww) a += *(const LAS f32x4*)(src + ww * (4 * 1056));
    const int cons = (br * 4 + cq) * 32 + bb * 8 + (quad >> 3);
    __builtin_amdgcn_raw_buffer_store_b128(nosent(a), rs, WS_OUT + cons * 4096 + h * 128 + (quad & 7) * 16, 0, AUX_SC1);
  }
  STAMP(9);
  if (w == 7) {
    const int io = WS_OUT + idx * 4096 + l * 16;
    v4u pv[4];
    for (unsigned spin = 0;; ++spin) {
      asm volatile("" ::: "memory");
#pragma unroll
      for (int n = 0; n < 4; ++n) pv[n] = __builtin_amdgcn_raw_buffer_load_b128(rs, io + n * 1024, 0, AUX_SC1);
      const bool bad = has_sent(pv[0]) | has_sent(pv[1]) | has_sent(pv[2]) | has_sent(pv[3]);
      if (__builtin_amdgcn_ballot_w64(bad) == 0ull || spin > SPIN_CAP) break;
      __builtin_amdgcn_s_sleep(1);
    }
    f32x4 a = (__builtin_bit_cast(f32x4, pv[0]) + __builtin_bit_cast(f32x4, pv[1])) +
              (__builtin_bit_cast(f32x4, pv[2]) + __builtin_bit_cast(f32x4, pv[3]));
#pragma unroll
    for (int k = 0; k < 4; ++k) { float t = a[k]; t += dpp<ROR8>(t); a[k] = x32_sum(x16_sum(t)); }
    const int r = (l >> 3) & 3;
    const float v = r == 0 ? a.x : r == 1 ? a.y : r == 2 ? a.z : a.w;
    const int obr = idx >> 7, ocq = (idx >> 5) & 3, ob = (idx >> 3) & 3, oc = idx & 7;
    if (l < 32) out[ob * 2048 + obr * 1024 + ocq * 256 + oc * 32 + l] = (unsigned short)(pk_bf16(v, v) & 0xFFFFu);
    STAMP(10);
  }
  STAMP(22);
  STAMP_FLUSH();
}

extern "C" void kernel_launch(void* const* d_in, const int* in_sizes, int n_in, void* d_out, int out_size, void* d_ws,
                              size_t ws_size, hipStream_t stream) {
  hipLaunchKernelGGL(fused_decode, dim3(256), dim3(576), 0, stream, (const char*)d_in[0], (const char*)d_in[1],
                     (const char*)d_in[2], (const char*)d_in[3], (const char*)d_in[4], (const char*)d_in[5],
                     (const char*)d_in[6], (const char*)d_in[7], (const char*)d_in[8], (const char*)d_in[9],
                     (unsigned short*)d_out, (char*)d_ws);
}
```

```cpp
#define DIAG 0
#include <hip/hip_runtime.h>
#include <stdint.h>

#define KV_AUX 2
#define W_AUX 2
#define WIN 4
#define XPT 8
#define PRETOUCH 1
#define STAMPS 0

typedef unsigned v4u __attribute__((ext_vector_type(4)));
typedef unsigned v2u __attribute__((ext_vector_type(2)));
typedef float f32x4 __attribute__((ext_vector_type(4)));
typedef __bf16 bf16x8 __attribute__((ext_vector_type(8)));
typedef short s16x4 __attribute__((ext_vector_type(4)));
#define GAS __attribute__((address_space(1)))
#define LAS __attribute__((address_space(3)))
#define SB() __builtin_amdgcn_sched_barrier(0)

constexpr int WS_EPOCH = 0;
constexpr int WS_FLAG = 4096;
constexpr int WS_QP = WS_FLAG + 256 * 128;
constexpr int WS_ML = WS_QP + 2 * 4 * 4 * 2 * 1024 * 4;
constexpr int WS_PO = WS_ML + 256 * 128;
constexpr int WS_OUT = WS_PO + 2 * 32 * 64 * 128 * 4;
constexpr int WS_END = WS_OUT + 256 * 32 * 32 * 4;
constexpr int WS_STAMP = WS_END;
constexpr int AUX_SC1 = 16;
constexpr unsigned SENT = 0xFFFFFFFFu;
constexpr unsigned SPIN_CAP = 1u << 16;

constexpr int L_SYNC = 0;
constexpr int L_WGT = 64;
constexpr int L_QRED = 512;
constexpr int L_QT = L_QRED + 16384;
constexpr int L_QS = L_QT + 1024;
constexpr int L_MX = L_QS + 2 * 8 * 272;
constexpr int L_LS = L_MX + 512;
constexpr int L_RED = L_LS + 512;
constexpr int L_RED2 = L_RED + 2 * 8 * 8 * 528;
constexpr int L_END = L_RED2 + 8 * 4 * 1056;
constexpr int S_QP = 0, S_Q = 4, S_MX = 8, S_LS = 12, S_W = 16;

#define MKRS(ptr, n) __builtin_amdgcn_make_buffer_rsrc((void*)(ptr), 0, (n), 0x00020000)
#define LDB(rs, off, aux) __builtin_amdgcn_raw_buffer_load_b128((rs), (off), 0, (aux))
__device__ __forceinline__ f32x4 mfma32(v4u a, v4u b, f32x4 c) {
  return __builtin_amdgcn_mfma_f32_16x16x32_bf16(__builtin_bit_cast(bf16x8, a), __builtin_bit_cast(bf16x8, b), c, 0, 0, 0);
}
__device__ __forceinline__ f32x4 mfma16(v2u a, v2u b, f32x4 c) {
  return __builtin_amdgcn_mfma_f32_16x16x16bf16_1k(__builtin_bit_cast(s16x4, a), __builtin_bit_cast(s16x4, b), c, 0, 0, 0);
}
__device__ __forceinline__ unsigned pk_bf16(float lo, float hi) {
  typedef __bf16 bf16x2 __attribute__((ext_vector_type(2)));
  typedef float f32x2 __attribute__((ext_vector_type(2)));
  f32x2 v = {lo, hi};
  return __builtin_bit_cast(unsigned, __builtin_convertvector(v, bf16x2));
}
__device__ __forceinline__ unsigned tr2(const v4u& r0, const v4u& r1, int dd) {
  return __builtin_amdgcn_perm(r1[dd >> 1], r0[dd >> 1], (dd & 1) ? 0x07060302u : 0x05040100u);
}
__device__ __forceinline__ float xg_max(float x) {
  auto s = __builtin_amdgcn_permlane16_swap(__float_as_uint(x), __float_as_uint(x), false, false);
  x = fmaxf(__uint_as_float(s[0]), __uint_as_float(s[1]));
  auto t = __builtin_amdgcn_permlane32_swap(__float_as_uint(x), __float_as_uint(x), false, false);
  return fmaxf(__uint_as_float(t[0]), __uint_as_float(t[1]));
}
__device__ __forceinline__ float x16_sum(float x) {
  auto s = __builtin_amdgcn_permlane16_swap(__float_as_uint(x), __float_as_uint(x), false, false);
  return __uint_as_float(s[0]) + __uint_as_float(s[1]);
}
__device__ __forceinline__ float x32_sum(float x) {
  auto t = __builtin_amdgcn_permlane32_swap(__float_as_uint(x), __float_as_uint(x), false, false);
  return __uint_as_float(t[0]) + __uint_as_float(t[1]);
}
template <int CTRL> __device__ __forceinline__ float dpp(float x) {
  return __builtin_bit_cast(float, __builtin_amdgcn_mov_dpp(__builtin_bit_cast(int, x), CTRL, 0xf, 0xf, true));
}
constexpr int ROR1 = 0x121, ROR2 = 0x122, ROR4 = 0x124, ROR8 = 0x128;
__device__ __forceinline__ v4u nosent(f32x4 v) {
  v4u u = __builtin_bit_cast(v4u, v);
  u.x = min(u.x, SENT - 1u); u.y = min(u.y, SENT - 1u); u.z = min(u.z, SENT - 1u); u.w = min(u.w, SENT - 1u);
  return u;
}
__device__ __forceinline__ bool has_sent(v4u u) { return max(max(u.x, u.y), max(u.z, u.w)) == SENT; }
__device__ __forceinline__ void lds_barrier() {
  asm volatile("s_waitcnt lgkmcnt(0)\n\ts_barrier" ::: "memory");
}
template <int N> __device__ __forceinline__ void waitvm() { asm volatile("s_waitcnt vmcnt(%0)" ::"i"(N) : "memory"); }
__device__ __forceinline__ void lds_signal(LAS char* L, int word, int l) {
  asm volatile("s_waitcnt lgkmcnt(0)" ::: "memory");
  if (l == 0) __hip_atomic_fetch_add((LAS unsigned*)(L + L_SYNC + word), 1u, __ATOMIC_RELAXED, __HIP_MEMORY_SCOPE_WORKGROUP);
  asm volatile("" ::: "memory");
}
__device__ __forceinline__ void lds_wait(LAS char* L, int word, unsigned want) {
  for (;;) {
    const unsigned v = __hip_atomic_load((LAS unsigned*)(L + L_SYNC + word), __ATOMIC_RELAXED, __HIP_MEMORY_SCOPE_WORKGROUP);
    if (__builtin_amdgcn_readfirstlane(v) >= want) break;
    __builtin_amdgcn_s_sleep(1);
  }
  asm volatile("" ::: "memory");
}

#define STAMP(n) do { if ((n) == 0 || (n) == DIAG) { SB(); ts[(n) == 0 ? 0 : 1] = __builtin_amdgcn_s_memrealtime(); SB(); } } while (0)
#define STAMP_FLUSH() do { if (w == 7) { const unsigned long long tgt = (DIAG ? ts[1] : ts[0]) + 4000ull; while (__builtin_amdgcn_s_memrealtime() < tgt) __builtin_amdgcn_s_sleep(2); } } while (0)

__global__ void __launch_bounds__(576, 3)
fused_decode(const char* __restrict__ xA, const char* __restrict__ kA, const char* __restrict__ vA, const char* __restrict__ wqA,
             const char* __restrict__ woA, const char* __restrict__ xB, const char* __restrict__ kB, const char* __restrict__ vB,
             const char* __restrict__ wqB, const char* __restrict__ woB, unsigned short* __restrict__ out, char* __restrict__ ws) {
  __shared__ __attribute__((aligned(16))) char smem[L_END];
  LAS char* L = (LAS char*)smem;
  const int c = blockIdx.x, tid = threadIdx.x, l = tid & 63, w = __builtin_amdgcn_readfirstlane(tid >> 6);
  const int idx = (c & 7) * 32 + (c >> 3), kvh = idx >> 6, gi = idx & 63;
  const int br = gi >> 5, cb = gi & 31;
  const int b = gi >> 4, s = gi & 15;
  const int h = kvh * 8 + ((gi >> 2) & 7), cq = gi & 3;
  const int g = l >> 4, rho = l & 15;
  const __amdgpu_buffer_rsrc_t rs = __builtin_amdgcn_make_buffer_rsrc((void*)ws, 0, WS_END, 0x00020000);
  unsigned long long ts[2] = {0, 0};
  STAMP(0);

  constexpr float C2 = 0.08838834764831845f * 1.4426950408889634f;

  if (w == 8) {
    __builtin_amdgcn_s_setprio(3);
    const unsigned long long epoch =
        __hip_atomic_load((GAS unsigned long long*)(ws + WS_EPOCH), __ATOMIC_RELAXED, __HIP_MEMORY_SCOPE_AGENT);
    if (l < 16) *(LAS unsigned*)(L + L_SYNC + l * 4) = 0u;
    unsigned touch = 0;
    {
      const size_t cub = (size_t)(b * 4 + kvh) * (8192 * 256) + (size_t)(128 * s) * 256;
      const int t = l & 15;
      const char* p = t < 4 ? kA + cub + t * (2048 * 256) : t < 8 ? vA + cub + (t - 4) * (2048 * 256)
                    : t == 8 ? kB + cub + 6144 * 256 : t == 9 ? vB + cub + 6144 * 256
                    : t < 14 ? (br ? woB : woA) + (size_t)(128 * h + 32 * (t - 10)) * 2048 + 512 * cq
                    : t == 14 ? (const char*)out : ws + WS_OUT + ((br * 4 + cq) * 32) * 4096;
      if (l < 16) touch = __builtin_nontemporal_load((const GAS unsigned*)p);
    }
    const int qslot = WS_QP + ((((br * 4 + kvh) * 4 + (l >> 4)) * 2 + (cb & 1)) * 1024 + (cb >> 1) * 64 + (l & 15) * 4) * 4;
    {
      const v4u sent = {SENT, SENT, SENT, SENT};
      __builtin_amdgcn_raw_buffer_store_b128(sent, rs, qslot, 0, AUX_SC1);
      const int po = WS_PO + ((br * 32 + h) * 64 + cq * 16) * 512 + l * 16;
#pragma unroll
      for (int n = 0; n < 8; ++n) __builtin_amdgcn_raw_buffer_store_b128(sent, rs, po + n * 1024, 0, AUX_SC1);
      const int oo = WS_OUT + idx * 4096 + l * 16;
#pragma unroll
      for (int n = 0; n < 4; ++n) __builtin_amdgcn_raw_buffer_store_b128(sent, rs, oo + n * 1024, 0, AUX_SC1);
      if (l < 8) __builtin_amdgcn_raw_buffer_store_b128(sent, rs, WS_ML + idx * 128 + l * 16, 0, AUX_SC1);
    }
    lds_barrier();
    const unsigned long long tag = epoch + 1ull;
    const unsigned tlo = (unsigned)tag, thi = (unsigned)(tag >> 32);
    waitvm<0>();
    asm volatile("" ::"v"(touch));
    if (l == 0) __builtin_amdgcn_raw_buffer_store_b64(v2u{tlo, thi}, rs, WS_FLAG + idx * 128, 0, AUX_SC1);
    STAMP(2);
    lds_wait(L, S_QP, 8);
    STAMP(1);
    {
      const LAS f32x4* src = (const LAS f32x4*)(L + L_QRED) + l;
      f32x4 a = src[0];
#pragma unroll
      for (int n = 1; n < 16; ++n) a += src[n * 64];
      LAS float* t = (LAS float*)(L + L_QT) + (l >> 3) * 32 + (l & 7);
      t[0] = a.x; t[8] = a.y; t[16] = a.z; t[24] = a.w;
      asm volatile("s_waitcnt lgkmcnt(0)" ::: "memory");
      a = *((const LAS f32x4*)(L + L_QT) + l);
      __builtin_amdgcn_raw_buffer_store_b128(nosent(a), rs, qslot, 0, AUX_SC1);
    }
    STAMP(16);
    {
      const int f1 = WS_FLAG + (kvh * 64 + l) * 128, f2 = WS_FLAG + ((br * 4 + cq) * 32 + (l & 31)) * 128;
      for (unsigned spin = 0;; ++spin) {
        asm volatile("" ::: "memory");
        const v2u x1 = __builtin_amdgcn_raw_buffer_load_b64(rs, f1, 0, AUX_SC1);
        const v2u x2 = __builtin_amdgcn_raw_buffer_load_b64(rs, f2, 0, AUX_SC1);
        if (__builtin_amdgcn_ballot_w64(((x1.x ^ tlo) | (x1.y ^ thi) | (x2.x ^ tlo) | (x2.y ^ thi)) != 0u) == 0ull || spin > SPIN_CAP) break;
        __builtin_amdgcn_s_sleep(8);
      }
      asm volatile("" ::: "memory");
    }
    STAMP(19);
    {
      const int qo = WS_QP + ((kvh * 4 + b) * 2 * 1024) * 4 + l * 16;
      v4u qg[16];
#pragma unroll
      for (int n = 0; n < 16; ++n) qg[n] = v4u{SENT, SENT, SENT, SENT};
      for (unsigned spin = 0;; ++spin) {
        asm volatile("" ::: "memory");
#pragma unroll
        for (int n = 0; n < 16; ++n)
          if (has_sent(qg[n])) qg[n] = __builtin_amdgcn_raw_buffer_load_b128(rs, qo + (n & 7) * 1024, (n >> 3) * (16 * 2 * 1024 * 4), AUX_SC1);
        bool bad = false;
#pragma unroll
        for (int n = 0; n < 16; ++n) bad |= has_sent(qg[n]);
        if (__builtin_amdgcn_ballot_w64(bad) == 0ull || spin > SPIN_CAP) break;
        __builtin_amdgcn_s_sleep(4);
      }
#pragma unroll
      for (int n = 0; n < 8; ++n) {
        const f32x4 q4 = __builtin_bit_cast(f32x4, qg[(n >> 2) * 8 + (n & 3)]) + __builtin_bit_cast(f32x4, qg[(n >> 2) * 8 + 4 + (n & 3)]);
        *(LAS v2u*)(L + L_QS + (2 * n + (l >> 5)) * 272 + (l & 31) * 8) = v2u{pk_bf16(q4.x, q4.y), pk_bf16(q4.z, q4.w)};
      }
    }
    lds_signal(L, S_Q, l);
    STAMP(3);
    if (idx == 0) {
      for (int pass = 0; pass < 4; ++pass)
        for (unsigned spin = 0;; ++spin) {
          asm volatile("" ::: "memory");
          const v2u x2 = __builtin_amdgcn_raw_buffer_load_b64(rs, WS_FLAG + (pass * 64 + l) * 128, 0, AUX_SC1);
          if (__builtin_amdgcn_ballot_w64(((x2.x ^ tlo) | (x2.y ^ thi)) != 0u) == 0ull || spin > SPIN_CAP) break;
          __builtin_amdgcn_s_sleep(8);
        }
      if (l == 0) __hip_atomic_store((GAS unsigned long long*)(ws + WS_EPOCH), tag, __ATOMIC_RELAXED, __HIP_MEMORY_SCOPE_AGENT);
    }
    lds_wait(L, S_LS, 8);
    if (l < 16) {
      const LAS f32x4* ls = (const LAS f32x4*)(L + L_LS + l * 32);
      const LAS f32x4* mx = (const LAS f32x4*)(L + L_MX + l * 32);
      const f32x4 l0 = ls[0], l1 = ls[1], m0 = mx[0], m1 = mx[1];
      const float lsum = ((l0.x + l0.y) + (l0.z + l0.w)) + ((l1.x + l1.y) + (l1.z + l1.w));
      const float mm = fmaxf(fmaxf(fmaxf(m0.x, m0.y), fmaxf(m0.z, m0.w)), fmaxf(fmaxf(m1.x, m1.y), fmaxf(m1.z, m1.w))) * C2;
      __builtin_amdgcn_raw_buffer_store_b64(v2u{min(__float_as_uint(mm), SENT - 1u), min(__float_as_uint(lsum), SENT - 1u)}, rs,
                                            WS_ML + idx * 128 + l * 8, 0, AUX_SC1);
    }
    STAMP(5);
    lds_barrier();
    {
      const int mo = WS_ML + (kvh * 64 + l) * 128 + (br * 8 + (h & 7)) * 8;
      v2u ml = {SENT, SENT};
      for (unsigned spin = 0;; ++spin) {
        asm volatile("" ::: "memory");
        if (max(ml.x, ml.y) == SENT) ml = __builtin_amdgcn_raw_buffer_load_b64(rs, mo, 0, AUX_SC1);
        if (__builtin_amdgcn_ballot_w64(max(ml.x, ml.y) == SENT) == 0ull || spin > SPIN_CAP) break;
        __builtin_amdgcn_s_sleep(4);
      }
      const float m = __uint_as_float(ml.x), ls = __uint_as_float(ml.y);
      float M = m;
      M = fmaxf(M, dpp<ROR8>(M)); M = fmaxf(M, dpp<ROR4>(M)); M = fmaxf(M, dpp<ROR2>(M)); M = fmaxf(M, dpp<ROR1>(M));
      const float e = __builtin_amdgcn_exp2f(m - M);
      float Ls = e * ls;
      Ls += dpp<ROR8>(Ls); Ls += dpp<ROR4>(Ls); Ls += dpp<ROR2>(Ls); Ls += dpp<ROR1>(Ls);
      *(LAS float*)(L + L_WGT + ((l >> 4) * 16 + (l & 3) * 4 + ((l >> 2) & 3)) * 4) = e / Ls;
    }
    lds_signal(L, S_W, l);
    STAMP(12);
    STAMP(22);
    STAMP_FLUSH();
    return;
  }

  const int qrow = 512 * (cb & 1) + 64 * w;
  const v4u xf = *(const GAS v4u*)((br ? xB : xA) + ((l & 3) * 1024 + qrow + 16 * g + 8 * ((l >> 3) & 1)) * 2);
  v4u wr[8];
  const __amdgpu_buffer_rsrc_t rwq = MKRS(br ? wqB : wqA, 8 << 20);
  const int wqp = (qrow + 8 * (l >> 3)) * 8192 + (kvh * 1024 + (cb >> 1) * 64) * 2 + (l & 7) * 16;
#pragma unroll
  for (int n = 0; n < WIN; ++n) wr[n] = LDB(rwq, wqp + n * 8192, W_AUX);
  SB();
  asm volatile("s_barrier" ::: "memory");
  SB();
#pragma unroll
  for (int n = WIN; n < 8; ++n) { waitvm<WIN - 1>(); wr[n] = LDB(rwq, wqp + n * 8192, W_AUX); SB(); }
  const int wbase = (b * 4 + kvh) * (8192 * 256) + (128 * s + 16 * w) * 256;
  const int koff = wbase + (4 * (rho & 3) + ((rho >> 2) & 1)) * 256 + g * 16 + 64 * (rho >> 3);
  const int voff = wbase + l * 16;
  constexpr int TS = 2048 * 256;
  v4u kf[20], vf[20], wo[8];
  const __amdgpu_buffer_rsrc_t rkA = MKRS(kA, 32 << 20), rkB = MKRS(kB, 32 << 20), rvA = MKRS(vA, 32 << 20), rvB = MKRS(vB, 32 << 20);
#define KLOAD(n) __builtin_amdgcn_raw_buffer_load_b128((n) < 16 ? rkA : rkB, koff + ((n) & 1) * 128 + (((n) >> 1) & 1) * 512, ((n) < 16 ? ((n) >> 2) : 3) * TS, KV_AUX)
#define VLOAD(n) __builtin_amdgcn_raw_buffer_load_b128((n) < 16 ? rvA : rvB, voff + ((n) & 3) * 1024, ((n) < 16 ? ((n) >> 2) : 3) * TS, KV_AUX)
#define PK(n) do { SB(); waitvm<WIN - 1>(); kf[n] = KLOAD(n); SB(); } while (0)
#define PV_(n) do { SB(); waitvm<WIN - 1>(); vf[n] = VLOAD(n); SB(); } while (0)
#define PVX(n) do { SB(); vf[n] = VLOAD(n); SB(); } while (0)
#pragma unroll
  for (int n = 0; n < WIN; ++n) PK(n);
  SB(); kf[WIN] = KLOAD(WIN); SB();

  {
    f32x4 qa[8];
#pragma unroll
    for (int dd = 0; dd < 8; ++dd) {
      const v4u a = {tr2(wr[0], wr[1], dd), tr2(wr[2], wr[3], dd), tr2(wr[4], wr[5], dd), tr2(wr[6], wr[7], dd)};
      qa[dd] = mfma32(a, xf, f32x4{0.f, 0.f, 0.f, 0.f});
    }
    if ((l & 12) == ((g >> 1) << 3)) {
      LAS f32x4* d = (LAS f32x4*)(L + L_QRED + ((w * 2 + (g >> 1)) * 4 + (l & 3)) * 256 + (g & 1) * 128);
#pragma unroll
      for (int dd = 0; dd < 8; ++dd) d[dd] = qa[dd];
    }
  }
  lds_signal(L, S_QP, l);
  STAMP(1);
#pragma unroll
  for (int n = WIN + 1; n < 20; ++n) PK(n);
#pragma unroll
  for (int n = 0; n < XPT; ++n) PV_(n);
  STAMP(4);
  lds_wait(L, S_Q, 1);
  STAMP(11);
  PVX(XPT);

  f32x4 sc[5];
  {
    const LAS char* qp = L + L_QS + (l & 7) * 272 + g * 16 + 64 * (rho >> 3);
    const v4u qa0 = *(const LAS v4u*)qp, qa1 = *(const LAS v4u*)(qp + 128);
    const v4u qb0 = *(const LAS v4u*)(qp + 8 * 272), qb1 = *(const LAS v4u*)(qp + 8 * 272 + 128);
#pragma unroll
    for (int u = 0; u < 5; ++u) {
      const f32x4 z = {0.f, 0.f, 0.f, 0.f};
      const f32x4 t1 = mfma32(kf[4 * u + 1], u < 4 ? qa1 : qb1, mfma32(kf[4 * u], u < 4 ? qa0 : qb0, z));
      const f32x4 t2 = mfma32(kf[4 * u + 3], u < 4 ? qa1 : qb1, mfma32(kf[4 * u + 2], u < 4 ? qa0 : qb0, z));
#pragma unroll
      for (int r = 0; r < 4; ++r) {
        auto sw = __builtin_amdgcn_permlane32_swap(__float_as_uint(t1[r]), __float_as_uint(t2[r]), false, false);
        sc[u][r] = __uint_as_float(sw[0]) + dpp<ROR8>(__uint_as_float(sw[1]));
      }
      if (u == 2) PVX(XPT + 1);
    }
  }
  {
    float mB = fmaxf(fmaxf(sc[4].x, sc[4].y), fmaxf(sc[4].z, sc[4].w));
    float mA = fmaxf(fmaxf(sc[0].x, sc[0].y), fmaxf(sc[0].z, sc[0].w));
#pragma unroll
    for (int u = 1; u < 4; ++u) mA = fmaxf(mA, fmaxf(fmaxf(sc[u].x, sc[u].y), fmaxf(sc[u].z, sc[u].w)));
    mA = xg_max(mA); mB = xg_max(mB);
    if (l < 8) {
      *(LAS float*)(L + L_MX + (l * 8 + w) * 4) = mA;
      *(LAS float*)(L + L_MX + 256 + (l * 8 + w) * 4) = mB;
    }
  }
  lds_signal(L, S_MX, l);
  STAMP(5);
  PV_(XPT + 2);
  PV_(XPT + 3);
  lds_wait(L, S_MX, 8);
  STAMP(12);
  PVX(XPT + 4);
  float MA, MB;
  {
    const LAS f32x4* m = (const LAS f32x4*)(L + L_MX + (l & 7) * 32);
    const f32x4 a0 = m[0], a1 = m[1], b0 = m[16], b1 = m[17];
    MA = fmaxf(fmaxf(fmaxf(a0.x, a0.y), fmaxf(a0.z, a0.w)), fmaxf(fmaxf(a1.x, a1.y), fmaxf(a1.z, a1.w))) * C2;
    MB = fmaxf(fmaxf(fmaxf(b0.x, b0.y), fmaxf(b0.z, b0.w)), fmaxf(fmaxf(b1.x, b1.y), fmaxf(b1.z, b1.w))) * C2;
  }
  unsigned pl[10], ph[10];
  {
    float la = 0.f, lb = 0.f;
#pragma unroll
    for (int u = 0; u < 5; ++u) {
      const float M = u < 4 ? MA : MB;
      const float p0 = __builtin_amdgcn_exp2f(fmaf(sc[u].x, C2, -M)), p1 = __builtin_amdgcn_exp2f(fmaf(sc[u].y, C2, -M));
      const float p2 = __builtin_amdgcn_exp2f(fmaf(sc[u].z, C2, -M)), p3 = __builtin_amdgcn_exp2f(fmaf(sc[u].w, C2, -M));
      const float t = (p0 + p1) + (p2 + p3);
      if (u < 4) la += t; else lb = t;
      pl[2 * u] = rho < 8 ? pk_bf16(p0, p1) : 0u; pl[2 * u + 1] = rho < 8 ? pk_bf16(p2, p3) : 0u;
      ph[2 * u] = __builtin_amdgcn_mov_dpp(pl[2 * u], ROR8, 0xf, 0xf, true);
      ph[2 * u + 1] = __builtin_amdgcn_mov_dpp(pl[2 * u + 1], ROR8, 0xf, 0xf, true);
      if (u == 2) PVX(XPT + 5);
    }
    la = x32_sum(x16_sum(la)); lb = x32_sum(x16_sum(lb));
    if (l < 8) {
      *(LAS float*)(L + L_LS + (l * 8 + w) * 4) = la;
      *(LAS float*)(L + L_LS + 256 + (l * 8 + w) * 4) = lb;
    }
  }
  lds_signal(L, S_LS, l);
  STAMP(13);
  PV_(XPT + 6);
  f32x4 acc[4];
#pragma unroll
  for (int q4 = 0; q4 < 4; ++q4) {
    const v4u a0 = {tr2(vf[0], vf[1], q4), tr2(vf[2], vf[3], q4), tr2(vf[4], vf[5], q4), tr2(vf[6], vf[7], q4)};
    const v4u a1 = {tr2(vf[0], vf[1], q4 + 4), tr2(vf[2], vf[3], q4 + 4), tr2(vf[4], vf[5], q4 + 4), tr2(vf[6], vf[7], q4 + 4)};
    acc[q4] = mfma32(a1, v4u{ph[0], ph[1], ph[2], ph[3]}, mfma32(a0, v4u{pl[0], pl[1], pl[2], pl[3]}, f32x4{0.f, 0.f, 0.f, 0.f}));
  }
#pragma unroll
  for (int n = XPT + 7; n < 20; ++n) PV_(n);
  const __amdgpu_buffer_rsrc_t rwo = MKRS(br ? woB : woA, 8 << 20);
  const int wop = (128 * h + 32 * (w >> 1) + 4 * (w & 1) + g) * 2048 + 512 * cq + rho * 16;
#define WLOAD(n) LDB(rwo, wop + ((n) >> 1) * (8 * 2048) + ((n) & 1) * 256, W_AUX)
#define PW(n) do { SB(); waitvm<WIN - 1>(); wo[n] = WLOAD(n); SB(); } while (0)
  SB(); wo[0] = WLOAD(0); SB();
  STAMP(14);
#pragma unroll
  for (int q4 = 0; q4 < 4; ++q4) {
    const v4u a0 = {tr2(vf[8], vf[9], q4), tr2(vf[10], vf[11], q4), tr2(vf[12], vf[13], q4), tr2(vf[14], vf[15], q4)};
    const v4u a1 = {tr2(vf[8], vf[9], q4 + 4), tr2(vf[10], vf[11], q4 + 4), tr2(vf[12], vf[13], q4 + 4), tr2(vf[14], vf[15], q4 + 4)};
    acc[q4] = mfma32(a1, v4u{ph[4], ph[5], ph[6], ph[7]}, mfma32(a0, v4u{pl[4], pl[5], pl[6], pl[7]}, acc[q4]));
  }
  LAS f32x4* dA = (LAS f32x4*)(L + L_RED + (w * 8 + (l & 7)) * 528 + g * 128 + (rho >> 3) * 64);
#pragma unroll
  for (int q4 = 0; q4 < 4; ++q4) dA[q4] = acc[q4];
  PW(1); PW(2); PW(3);
  STAMP(20);
#pragma unroll
  for (int q4 = 0; q4 < 4; ++q4) {
    const v2u a0 = {tr2(vf[16], vf[17], q4), tr2(vf[18], vf[19], q4)};
    const v2u a1 = {tr2(vf[16], vf[17], q4 + 4), tr2(vf[18], vf[19], q4 + 4)};
    dA[8 * 8 * 33 + q4] = mfma16(a1, v2u{ph[8], ph[9]}, mfma16(a0, v2u{pl[8], pl[9]}, f32x4{0.f, 0.f, 0.f, 0.f}));
  }
  SB(); wo[4] = WLOAD(4); SB();
  STAMP(6);
  lds_barrier();
  STAMP(15);
  {
    const int pb = tid >> 8, hd = (tid >> 5) & 7, quad = tid & 31;
    const LAS char* src = L + L_RED + pb * (8 * 8 * 528) + hd * 528 + quad * 16;
    f32x4 a = *(const LAS f32x4*)src;
#pragma unroll
    for (int ww = 1; ww < 8; ++ww) a += *(const LAS f32x4*)(src + ww * (8 * 528));
    const int rec = (pb * 32 + kvh * 8 + hd) * 64 + b * 16 + s;
    __builtin_amdgcn_raw_buffer_store_b128(nosent(a), rs, WS_PO + rec * 512 + quad * 16, 0, AUX_SC1);
  }
  STAMP(7);
  SB();
#pragma unroll
  for (int n = 5; n < 8; ++n) wo[n] = WLOAD(n);
  SB();
  v2u wt[16];
#pragma unroll
  for (int m = 0; m < 16; ++m) {
    wt[m] = v2u{tr2(wo[m >> 3], wo[2 + (m >> 3)], m & 7), tr2(wo[4 + (m >> 3)], wo[6 + (m >> 3)], m & 7)};
    asm volatile("" : "+v"(wt[m]));
  }
  const int sg = (l >> 2) & 3;
  STAMP(21);
  SB();

  {
    const int po = WS_PO + ((br * 32 + h) * 64 + (l & 3) * 16 + sg) * 512 + (16 * w + 4 * g) * 4;
    v4u pv[4];
#pragma unroll
    for (int n = 0; n < 4; ++n) pv[n] = __builtin_amdgcn_raw_buffer_load_b128(rs, po + n * 2048, 0, AUX_SC1);
    SB();
    lds_wait(L, S_W, 1);
    const f32x4 wg = *(const LAS f32x4*)(L + L_WGT + ((l & 3) * 16 + sg * 4) * 4);
    SB();
    for (unsigned spin = 0;; ++spin) {
      const bool bad = has_sent(pv[0]) | has_sent(pv[1]) | has_sent(pv[2]) | has_sent(pv[3]);
      if (__builtin_amdgcn_ballot_w64(bad) == 0ull || spin > SPIN_CAP) break;
      asm volatile("" ::: "memory");
      if (bad) {
#pragma unroll
        for (int n = 0; n < 4; ++n) pv[n] = __builtin_amdgcn_raw_buffer_load_b128(rs, po + n * 2048, 0, AUX_SC1);
      }
    }
    STAMP(8);
    f32x4 o = __builtin_bit_cast(f32x4, pv[0]) * wg.x + __builtin_bit_cast(f32x4, pv[1]) * wg.y +
              (__builtin_bit_cast(f32x4, pv[2]) * wg.z + __builtin_bit_cast(f32x4, pv[3]) * wg.w);
#pragma unroll
    for (int k = 0; k < 4; ++k) { float t = o[k]; t += dpp<ROR8>(t); t += dpp<ROR4>(t); o[k] = t; }
    const unsigned o01 = pk_bf16(o.x, o.y), o23 = pk_bf16(o.z, o.w);
    v2u bm[4];
#pragma unroll
    for (int k = 0; k < 4; ++k) bm[k] = v2u{sg == k ? o01 : 0u, sg == k ? o23 : 0u};
    LAS f32x4* d = (LAS f32x4*)(L + L_RED2 + (w * 4 + (l & 3)) * 1056 + g * 128 + sg * 16);
#pragma unroll
    for (int a = 0; a < 4; ++a) {
      f32x4 z = {0.f, 0.f, 0.f, 0.f};
#pragma unroll
      for (int k = 0; k < 4; ++k) z = mfma16(wt[4 * a + k], bm[k], z);
      d[(a >> 1) * 32 + (a & 1) * 4] = z;
    }
  }
  STAMP(18);
  lds_barrier();
  if (tid < 256) {
    const int bb = tid >> 6, quad = tid & 63;
    const LAS char* src = L + L_RED2 + bb * 1056 + quad * 16;
    f32x4 a = *(const LAS f32x4*)src;
#pragma unroll
    for (int ww = 1; ww < 8; ++ww) a += *(const LAS f32x4*)(src + ww * (4 * 1056));
    const int cons = (br * 4 + cq) * 32 + bb * 8 + (quad >> 3);
    __builtin_amdgcn_raw_buffer_store_b128(nosent(a), rs, WS_OUT + cons * 4096 + h * 128 + (quad & 7) * 16, 0, AUX_SC1);
  }
  STAMP(9);
  if (w == 7) {
    const int io = WS_OUT + idx * 4096 + l * 16;
    v4u pv[4];
    for (unsigned spin = 0;; ++spin) {
      asm volatile("" ::: "memory");
#pragma unroll
      for (int n = 0; n < 4; ++n) pv[n] = __builtin_amdgcn_raw_buffer_load_b128(rs, io + n * 1024, 0, AUX_SC1);
      const bool bad = has_sent(pv[0]) | has_sent(pv[1]) | has_sent(pv[2]) | has_sent(pv[3]);
      if (__builtin_amdgcn_ballot_w64(bad) == 0ull || spin > SPIN_CAP) break;
      __builtin_amdgcn_s_sleep(1);
    }
    f32x4 a = (__builtin_bit_cast(f32x4, pv[0]) + __builtin_bit_cast(f32x4, pv[1])) +
              (__builtin_bit_cast(f32x4, pv[2]) + __builtin_bit_cast(f32x4, pv[3]));
#pragma unroll
    for (int k = 0; k < 4; ++k) { float t = a[k]; t += dpp<ROR8>(t); a[k] = x32_sum(x16_sum(t)); }
    const int r = (l >> 3) & 3;
    const float v = r == 0 ? a.x : r == 1 ? a.y : r == 2 ? a.z : a.w;
    const int obr = idx >> 7, ocq = (idx >> 5) & 3, ob = (idx >> 3) & 3, oc = idx & 7;
    if (l < 32) out[ob * 2048 + obr * 1024 + ocq * 256 + oc * 32 + l] = (unsigned short)(pk_bf16(v, v) & 0xFFFFu);
    STAMP(10);
  }
  STAMP(22);
  STAMP_FLUSH();
}

extern "C" void kernel_launch(void* const* d_in, const int* in_sizes, int n_in, void* d_out, int out_size, void* d_ws,
                              size_t ws_size, hipStream_t stream) {
  hipLaunchKernelGGL(fused_decode, dim3(256), dim3(576), 0, stream, (const char*)d_in[0], (const char*)d_in[1],
                     (const char*)d_in[2], (const char*)d_in[3], (const char*)d_in[4], (const char*)d_in[5],
                     (const char*)d_in[6], (const char*)d_in[7], (const char*)d_in[8], (const char*)d_in[9],
                     (unsigned short*)d_out, (char*)d_ws);
}
```

```cpp
#define USEMAX 0
#define SLP 2
#define DIAG 0
#include <hip/hip_runtime.h>
#include <stdint.h>

#define KV_AUX 2
#define W_AUX 2
#define WIN 4
#define XPT 8
#define PRETOUCH 1
#define STAMPS 0

typedef unsigned v4u __attribute__((ext_vector_type(4)));
typedef unsigned v2u __attribute__((ext_vector_type(2)));
typedef float f32x4 __attribute__((ext_vector_type(4)));
typedef __bf16 bf16x8 __attribute__((ext_vector_type(8)));
typedef short s16x4 __attribute__((ext_vector_type(4)));
#define GAS __attribute__((address_space(1)))
#define LAS __attribute__((address_space(3)))
#define SB() __builtin_amdgcn_sched_barrier(0)

constexpr int WS_EPOCH = 0;
constexpr int WS_FLAG = 4096;
constexpr int WS_QP = WS_FLAG + 256 * 128;
constexpr int WS_ML = WS_QP + 2 * 4 * 4 * 2 * 1024 * 4;
constexpr int WS_PO = WS_ML + 256 * 128;
constexpr int WS_OUT = WS_PO + 2 * 32 * 64 * 128 * 4;
constexpr int WS_END = WS_OUT + 256 * 32 * 32 * 4;
constexpr int WS_STAMP = WS_END;
constexpr int AUX_SC1 = 16;
constexpr unsigned SENT = 0xFFFFFFFFu;
constexpr unsigned SPIN_CAP = 1u << 16;

constexpr int L_SYNC = 0;
constexpr int L_WGT = 64;
constexpr int L_QRED = 512;
constexpr int L_QT = L_QRED + 16384;
constexpr int L_QS = L_QT + 1024;
constexpr int L_MX = L_QS + 2 * 8 * 272;
constexpr int L_LS = L_MX + 512;
constexpr int L_RED = L_LS + 512;
constexpr int L_RED2 = L_RED + 2 * 8 * 8 * 528;
constexpr int L_END = L_RED2 + 8 * 4 * 1056;
constexpr int S_QP = 0, S_Q = 4, S_MX = 8, S_LS = 12, S_W = 16;

#define MKRS(ptr, n) __builtin_amdgcn_make_buffer_rsrc((void*)(ptr), 0, (n), 0x00020000)
#define LDB(rs, off, aux) __builtin_amdgcn_raw_buffer_load_b128((rs), (off), 0, (aux))
__device__ __forceinline__ f32x4 mfma32(v4u a, v4u b, f32x4 c) {
  return __builtin_amdgcn_mfma_f32_16x16x32_bf16(__builtin_bit_cast(bf16x8, a), __builtin_bit_cast(bf16x8, b), c, 0, 0, 0);
}
__device__ __forceinline__ f32x4 mfma16(v2u a, v2u b, f32x4 c) {
  return __builtin_amdgcn_mfma_f32_16x16x16bf16_1k(__builtin_bit_cast(s16x4, a), __builtin_bit_cast(s16x4, b), c, 0, 0, 0);
}
__device__ __forceinline__ unsigned pk_bf16(float lo, float hi) {
  typedef __bf16 bf16x2 __attribute__((ext_vector_type(2)));
  typedef float f32x2 __attribute__((ext_vector_type(2)));
  f32x2 v = {lo, hi};
  return __builtin_bit_cast(unsigned, __builtin_convertvector(v, bf16x2));
}
__device__ __forceinline__ unsigned tr2(const v4u& r0, const v4u& r1, int dd) {
  return __builtin_amdgcn_perm(r1[dd >> 1], r0[dd >> 1], (dd & 1) ? 0x07060302u : 0x05040100u);
}
__device__ __forceinline__ float xg_max(float x) {
  auto s = __builtin_amdgcn_permlane16_swap(__float_as_uint(x), __float_as_uint(x), false, false);
  x = fmaxf(__uint_as_float(s[0]), __uint_as_float(s[1]));
  auto t = __builtin_amdgcn_permlane32_swap(__float_as_uint(x), __float_as_uint(x), false, false);
  return fmaxf(__uint_as_float(t[0]), __uint_as_float(t[1]));
}
__device__ __forceinline__ float x16_sum(float x) {
  auto s = __builtin_amdgcn_permlane16_swap(__float_as_uint(x), __float_as_uint(x), false, false);
  return __uint_as_float(s[0]) + __uint_as_float(s[1]);
}
__device__ __forceinline__ float x32_sum(float x) {
  auto t = __builtin_amdgcn_permlane32_swap(__float_as_uint(x), __float_as_uint(x), false, false);
  return __uint_as_float(t[0]) + __uint_as_float(t[1]);
}
template <int CTRL> __device__ __forceinline__ float dpp(float x) {
  return __builtin_bit_cast(float, __builtin_amdgcn_mov_dpp(__builtin_bit_cast(int, x), CTRL, 0xf, 0xf, true));
}
constexpr int ROR1 = 0x121, ROR2 = 0x122, ROR4 = 0x124, ROR8 = 0x128;
__device__ __forceinline__ v4u nosent(f32x4 v) {
  v4u u = __builtin_bit_cast(v4u, v);
  u.x = min(u.x, SENT - 1u); u.y = min(u.y, SENT - 1u); u.z = min(u.z, SENT - 1u); u.w = min(u.w, SENT - 1u);
  return u;
}
__device__ __forceinline__ bool has_sent(v4u u) { return max(max(u.x, u.y), max(u.z, u.w)) == SENT; }
__device__ __forceinline__ void lds_barrier() {
  asm volatile("s_waitcnt lgkmcnt(0)\n\ts_barrier" ::: "memory");
}
template <int N> __device__ __forceinline__ void waitvm() { asm volatile("s_waitcnt vmcnt(%0)" ::"i"(N) : "memory"); }
__device__ __forceinline__ void lds_signal(LAS char* L, int word, int l) {
  asm volatile("s_waitcnt lgkmcnt(0)" ::: "memory");
  if (l == 0) __hip_atomic_fetch_add((LAS unsigned*)(L + L_SYNC + word), 1u, __ATOMIC_RELAXED, __HIP_MEMORY_SCOPE_WORKGROUP);
  asm volatile("" ::: "memory");
}
__device__ __forceinline__ void lds_wait(LAS char* L, int word, unsigned want) {
  for (;;) {
    const unsigned v = __hip_atomic_load((LAS unsigned*)(L + L_SYNC + word), __ATOMIC_RELAXED, __HIP_MEMORY_SCOPE_WORKGROUP);
    if (__builtin_amdgcn_readfirstlane(v) >= want) break;
    __builtin_amdgcn_s_sleep(1);
  }
  asm volatile("" ::: "memory");
}

#define STAMP(n) do { if ((n) == 0 || (n) == DIAG) { SB(); ts[(n) == 0 ? 0 : 1] = __builtin_amdgcn_s_memrealtime(); SB(); } } while (0)
#define STAMP_FLUSH() do { if (w == 8) { if (l == 0) *(LAS unsigned long long*)(L + 32) = hold; } } while (0)

__global__ void __launch_bounds__(576, 3)
fused_decode(const char* __restrict__ xA, const char* __restrict__ kA, const char* __restrict__ vA, const char* __restrict__ wqA,
             const char* __restrict__ woA, const char* __restrict__ xB, const char* __restrict__ kB, const char* __restrict__ vB,
             const char* __restrict__ wqB, const char* __restrict__ woB, unsigned short* __restrict__ out, char* __restrict__ ws) {
  __shared__ __attribute__((aligned(16))) char smem[L_END];
  LAS char* L = (LAS char*)smem;
  const int c = blockIdx.x, tid = threadIdx.x, l = tid & 63, w = __builtin_amdgcn_readfirstlane(tid >> 6);
  const int idx = (c & 7) * 32 + (c >> 3), kvh = idx >> 6, gi = idx & 63;
  const int br = gi >> 5, cb = gi & 31;
  const int b = gi >> 4, s = gi & 15;
  const int h = kvh * 8 + ((gi >> 2) & 7), cq = gi & 3;
  const int g = l >> 4, rho = l & 15;
  const __amdgpu_buffer_rsrc_t rs = __builtin_amdgcn_make_buffer_rsrc((void*)ws, 0, WS_END, 0x00020000);
  unsigned long long ts[2] = {0, 0}; unsigned long long hold = 0;
  STAMP(0);

  constexpr float C2 = 0.08838834764831845f * 1.4426950408889634f;

  if (w == 8) {
    __builtin_amdgcn_s_setprio(3);
    const unsigned long long epoch =
        __hip_atomic_load((GAS unsigned long long*)(ws + WS_EPOCH), __ATOMIC_RELAXED, __HIP_MEMORY_SCOPE_AGENT);
    if (l < 16) *(LAS unsigned*)(L + L_SYNC + l * 4) = 0u;
    unsigned touch = 0;
    {
      const size_t cub = (size_t)(b * 4 + kvh) * (8192 * 256) + (size_t)(128 * s) * 256;
      const int t = l & 15;
      const char* p = t < 4 ? kA + cub + t * (2048 * 256) : t < 8 ? vA + cub + (t - 4) * (2048 * 256)
                    : t == 8 ? kB + cub + 6144 * 256 : t == 9 ? vB + cub + 6144 * 256
                    : t < 14 ? (br ? woB : woA) + (size_t)(128 * h + 32 * (t - 10)) * 2048 + 512 * cq
                    : t == 14 ? (const char*)out : ws + WS_OUT + ((br * 4 + cq) * 32) * 4096;
      if (l < 16) touch = __builtin_nontemporal_load((const GAS unsigned*)p);
    }
    const int qslot = WS_QP + ((((br * 4 + kvh) * 4 + (l >> 4)) * 2 + (cb & 1)) * 1024 + (cb >> 1) * 64 + (l & 15) * 4) * 4;
    {
      const v4u sent = {SENT, SENT, SENT, SENT};
      __builtin_amdgcn_raw_buffer_store_b128(sent, rs, qslot, 0, AUX_SC1);
      const int po = WS_PO + ((br * 32 + h) * 64 + cq * 16) * 512 + l * 16;
#pragma unroll
      for (int n = 0; n < 8; ++n) __builtin_amdgcn_raw_buffer_store_b128(sent, rs, po + n * 1024, 0, AUX_SC1);
      const int oo = WS_OUT + idx * 4096 + l * 16;
#pragma unroll
      for (int n = 0; n < 4; ++n) __builtin_amdgcn_raw_buffer_store_b128(sent, rs, oo + n * 1024, 0, AUX_SC1);
      if (l < 8) __builtin_amdgcn_raw_buffer_store_b128(sent, rs, WS_ML + idx * 128 + l * 16, 0, AUX_SC1);
    }
    lds_barrier();
    const unsigned long long tag = epoch + 1ull;
    const unsigned tlo = (unsigned)tag, thi = (unsigned)(tag >> 32);
    waitvm<0>();
    asm volatile("" ::"v"(touch));
    if (l == 0) __builtin_amdgcn_raw_buffer_store_b128(v4u{tlo, thi, (unsigned)ts[0], (unsigned)(ts[0] >> 32)}, rs, WS_FLAG + idx * 128, 0, AUX_SC1);
    STAMP(2);
    lds_wait(L, S_QP, 8);
    STAMP(1);
    {
      const LAS f32x4* src = (const LAS f32x4*)(L + L_QRED) + l;
      f32x4 a = src[0];
#pragma unroll
      for (int n = 1; n < 16; ++n) a += src[n * 64];
      LAS float* t = (LAS float*)(L + L_QT) + (l >> 3) * 32 + (l & 7);
      t[0] = a.x; t[8] = a.y; t[16] = a.z; t[24] = a.w;
      asm volatile("s_waitcnt lgkmcnt(0)" ::: "memory");
      a = *((const LAS f32x4*)(L + L_QT) + l);
      __builtin_amdgcn_raw_buffer_store_b128(nosent(a), rs, qslot, 0, AUX_SC1);
    }
    STAMP(16);
    {
      const int f1 = WS_FLAG + (kvh * 64 + l) * 128, f2 = WS_FLAG + ((br * 4 + cq) * 32 + (l & 31)) * 128;
      for (unsigned spin = 0;; ++spin) {
        asm volatile("" ::: "memory");
        const v2u x1 = __builtin_amdgcn_raw_buffer_load_b64(rs, f1, 0, AUX_SC1);
        const v2u x2 = __builtin_amdgcn_raw_buffer_load_b64(rs, f2, 0, AUX_SC1);
        if (__builtin_amdgcn_ballot_w64(((x1.x ^ tlo) | (x1.y ^ thi) | (x2.x ^ tlo) | (x2.y ^ thi)) != 0u) == 0ull || spin > SPIN_CAP) break;
        __builtin_amdgcn_s_sleep(8);
      }
      asm volatile("" ::: "memory");
    }
    STAMP(19);
    {
      const int qo = WS_QP + ((kvh * 4 + b) * 2 * 1024) * 4 + l * 16;
      v4u qg[16];
#pragma unroll
      for (int n = 0; n < 16; ++n) qg[n] = v4u{SENT, SENT, SENT, SENT};
      for (unsigned spin = 0;; ++spin) {
        asm volatile("" ::: "memory");
#pragma unroll
        for (int n = 0; n < 16; ++n)
          if (has_sent(qg[n])) qg[n] = __builtin_amdgcn_raw_buffer_load_b128(rs, qo + (n & 7) * 1024, (n >> 3) * (16 * 2 * 1024 * 4), AUX_SC1);
        bool bad = false;
#pragma unroll
        for (int n = 0; n < 16; ++n) bad |= has_sent(qg[n]);
        if (__builtin_amdgcn_ballot_w64(bad) == 0ull || spin > SPIN_CAP) break;
        __builtin_amdgcn_s_sleep(4);
      }
#pragma unroll
      for (int n = 0; n < 8; ++n) {
        const f32x4 q4 = __builtin_bit_cast(f32x4, qg[(n >> 2) * 8 + (n & 3)]) + __builtin_bit_cast(f32x4, qg[(n >> 2) * 8 + 4 + (n & 3)]);
        *(LAS v2u*)(L + L_QS + (2 * n + (l >> 5)) * 272 + (l & 31) * 8) = v2u{pk_bf16(q4.x, q4.y), pk_bf16(q4.z, q4.w)};
      }
    }
    lds_signal(L, S_Q, l);
    STAMP(3);
    if (idx == 0) {
      for (int pass = 0; pass < 4; ++pass)
        for (unsigned spin = 0;; ++spin) {
          asm volatile("" ::: "memory");
          const v2u x2 = __builtin_amdgcn_raw_buffer_load_b64(rs, WS_FLAG + (pass * 64 + l) * 128, 0, AUX_SC1);
          if (__builtin_amdgcn_ballot_w64(((x2.x ^ tlo) | (x2.y ^ thi)) != 0u) == 0ull || spin > SPIN_CAP) break;
          __builtin_amdgcn_s_sleep(8);
        }
      {
        int dmin = 1 << 30, dmax = -(1 << 30);
        for (int pass = 0; pass < 4; ++pass) {
          const v4u x2 = __builtin_amdgcn_raw_buffer_load_b128(rs, WS_FLAG + (pass * 64 + l) * 128, 0, AUX_SC1);
          const int d = (int)(x2.z - (unsigned)ts[0]);
          dmin = min(dmin, d); dmax = max(dmax, d);
        }
        for (int o = 1; o < 64; o <<= 1) { dmin = min(dmin, __shfl_xor(dmin, o)); dmax = max(dmax, __shfl_xor(dmax, o)); }
        hold = ts[0] + (long long)__builtin_amdgcn_readfirstlane(USEMAX ? dmax : dmin) + 4000ull;
      }
      if (l == 0) __hip_atomic_store((GAS unsigned long long*)(ws + WS_EPOCH), tag, __ATOMIC_RELAXED, __HIP_MEMORY_SCOPE_AGENT);
    }
    lds_wait(L, S_LS, 8);
    if (l < 16) {
      const LAS f32x4* ls = (const LAS f32x4*)(L + L_LS + l * 32);
      const LAS f32x4* mx = (const LAS f32x4*)(L + L_MX + l * 32);
      const f32x4 l0 = ls[0], l1 = ls[1], m0 = mx[0], m1 = mx[1];
      const float lsum = ((l0.x + l0.y) + (l0.z + l0.w)) + ((l1.x + l1.y) + (l1.z + l1.w));
      const float mm = fmaxf(fmaxf(fmaxf(m0.x, m0.y), fmaxf(m0.z, m0.w)), fmaxf(fmaxf(m1.x, m1.y), fmaxf(m1.z, m1.w))) * C2;
      __builtin_amdgcn_raw_buffer_store_b64(v2u{min(__float_as_uint(mm), SENT - 1u), min(__float_as_uint(lsum), SENT - 1u)}, rs,
                                            WS_ML + idx * 128 + l * 8, 0, AUX_SC1);
    }
    STAMP(5);
    lds_barrier();
    {
      const int mo = WS_ML + (kvh * 64 + l) * 128 + (br * 8 + (h & 7)) * 8;
      v2u ml = {SENT, SENT};
      for (unsigned spin = 0;; ++spin) {
        asm volatile("" ::: "memory");
        if (max(ml.x, ml.y) == SENT) ml = __builtin_amdgcn_raw_buffer_load_b64(rs, mo, 0, AUX_SC1);
        if (__builtin_amdgcn_ballot_w64(max(ml.x, ml.y) == SENT) == 0ull || spin > SPIN_CAP) break;
        __builtin_amdgcn_s_sleep(4);
      }
      const float m = __uint_as_float(ml.x), ls = __uint_as_float(ml.y);
      float M = m;
      M = fmaxf(M, dpp<ROR8>(M)); M = fmaxf(M, dpp<ROR4>(M)); M = fmaxf(M, dpp<ROR2>(M)); M = fmaxf(M, dpp<ROR1>(M));
      const float e = __builtin_amdgcn_exp2f(m - M);
      float Ls = e * ls;
      Ls += dpp<ROR8>(Ls); Ls += dpp<ROR4>(Ls); Ls += dpp<ROR2>(Ls); Ls += dpp<ROR1>(Ls);
      *(LAS float*)(L + L_WGT + ((l >> 4) * 16 + (l & 3) * 4 + ((l >> 2) & 3)) * 4) = e / Ls;
    }
    lds_signal(L, S_W, l);
    STAMP(12);
    STAMP(22);
    STAMP_FLUSH();
    return;
  }

  const int qrow = 512 * (cb & 1) + 64 * w;
  const v4u xf = *(const GAS v4u*)((br ? xB : xA) + ((l & 3) * 1024 + qrow + 16 * g + 8 * ((l >> 3) & 1)) * 2);
  v4u wr[8];
  const __amdgpu_buffer_rsrc_t rwq = MKRS(br ? wqB : wqA, 8 << 20);
  const int wqp = (qrow + 8 * (l >> 3)) * 8192 + (kvh * 1024 + (cb >> 1) * 64) * 2 + (l & 7) * 16;
#pragma unroll
  for (int n = 0; n < WIN; ++n) wr[n] = LDB(rwq, wqp + n * 8192, W_AUX);
  SB();
  asm volatile("s_barrier" ::: "memory");
  SB();
#pragma unroll
  for (int n = WIN; n < 8; ++n) { waitvm<WIN - 1>(); wr[n] = LDB(rwq, wqp + n * 8192, W_AUX); SB(); }
  const int wbase = (b * 4 + kvh) * (8192 * 256) + (128 * s + 16 * w) * 256;
  const int koff = wbase + (4 * (rho & 3) + ((rho >> 2) & 1)) * 256 + g * 16 + 64 * (rho >> 3);
  const int voff = wbase + l * 16;
  constexpr int TS = 2048 * 256;
  v4u kf[20], vf[20], wo[8];
  const __amdgpu_buffer_rsrc_t rkA = MKRS(kA, 32 << 20), rkB = MKRS(kB, 32 << 20), rvA = MKRS(vA, 32 << 20), rvB = MKRS(vB, 32 << 20);
#define KLOAD(n) __builtin_amdgcn_raw_buffer_load_b128((n) < 16 ? rkA : rkB, koff + ((n) & 1) * 128 + (((n) >> 1) & 1) * 512, ((n) < 16 ? ((n) >> 2) : 3) * TS, KV_AUX)
#define VLOAD(n) __builtin_amdgcn_raw_buffer_load_b128((n) < 16 ? rvA : rvB, voff + ((n) & 3) * 1024, ((n) < 16 ? ((n) >> 2) : 3) * TS, KV_AUX)
#define PK(n) do { SB(); waitvm<WIN - 1>(); kf[n] = KLOAD(n); SB(); } while (0)
#define PV_(n) do { SB(); waitvm<WIN - 1>(); vf[n] = VLOAD(n); SB(); } while (0)
#define PVX(n) do { SB(); vf[n] = VLOAD(n); SB(); } while (0)
#pragma unroll
  for (int n = 0; n < WIN; ++n) PK(n);
  SB(); kf[WIN] = KLOAD(WIN); SB();

  {
    f32x4 qa[8];
#pragma unroll
    for (int dd = 0; dd < 8; ++dd) {
      const v4u a = {tr2(wr[0], wr[1], dd), tr2(wr[2], wr[3], dd), tr2(wr[4], wr[5], dd), tr2(wr[6], wr[7], dd)};
      qa[dd] = mfma32(a, xf, f32x4{0.f, 0.f, 0.f, 0.f});
    }
    if ((l & 12) == ((g >> 1) << 3)) {
      LAS f32x4* d = (LAS f32x4*)(L + L_QRED + ((w * 2 + (g >> 1)) * 4 + (l & 3)) * 256 + (g & 1) * 128);
#pragma unroll
      for (int dd = 0; dd < 8; ++dd) d[dd] = qa[dd];
    }
  }
  lds_signal(L, S_QP, l);
  STAMP(1);
#pragma unroll
  for (int n = WIN + 1; n < 20; ++n) PK(n);
#pragma unroll
  for (int n = 0; n < XPT; ++n) PV_(n);
  STAMP(4);
  lds_wait(L, S_Q, 1);
  STAMP(11);
  PVX(XPT);

  f32x4 sc[5];
  {
    const LAS char* qp = L + L_QS + (l & 7) * 272 + g * 16 + 64 * (rho >> 3);
    const v4u qa0 = *(const LAS v4u*)qp, qa1 = *(const LAS v4u*)(qp + 128);
    const v4u qb0 = *(const LAS v4u*)(qp + 8 * 272), qb1 = *(const LAS v4u*)(qp + 8 * 272 + 128);
#pragma unroll
    for (int u = 0; u < 5; ++u) {
      const f32x4 z = {0.f, 0.f, 0.f, 0.f};
      const f32x4 t1 = mfma32(kf[4 * u + 1], u < 4 ? qa1 : qb1, mfma32(kf[4 * u], u < 4 ? qa0 : qb0, z));
      const f32x4 t2 = mfma32(kf[4 * u + 3], u < 4 ? qa1 : qb1, mfma32(kf[4 * u + 2], u < 4 ? qa0 : qb0, z));
#pragma unroll
      for (int r = 0; r < 4; ++r) {
        auto sw = __builtin_amdgcn_permlane32_swap(__float_as_uint(t1[r]), __float_as_uint(t2[r]), false, false);
        sc[u][r] = __uint_as_float(sw[0]) + dpp<ROR8>(__uint_as_float(sw[1]));
      }
      if (u == 2) PVX(XPT + 1);
    }
  }
  {
    float mB = fmaxf(fmaxf(sc[4].x, sc[4].y), fmaxf(sc[4].z, sc[4].w));
    float mA = fmaxf(fmaxf(sc[0].x, sc[0].y), fmaxf(sc[0].z, sc[0].w));
#pragma unroll
    for (int u = 1; u < 4; ++u) mA = fmaxf(mA, fmaxf(fmaxf(sc[u].x, sc[u].y), fmaxf(sc[u].z, sc[u].w)));
    mA = xg_max(mA); mB = xg_max(mB);
    if (l < 8) {
      *(LAS float*)(L + L_MX + (l * 8 + w) * 4) = mA;
      *(LAS float*)(L + L_MX + 256 + (l * 8 + w) * 4) = mB;
    }
  }
  lds_signal(L, S_MX, l);
  STAMP(5);
  PV_(XPT + 2);
  PV_(XPT + 3);
  lds_wait(L, S_MX, 8);
  STAMP(12);
  PVX(XPT + 4);
  float MA, MB;
  {
    const LAS f32x4* m = (const LAS f32x4*)(L + L_MX + (l & 7) * 32);
    const f32x4 a0 = m[0], a1 = m[1], b0 = m[16], b1 = m[17];
    MA = fmaxf(fmaxf(fmaxf(a0.x, a0.y), fmaxf(a0.z, a0.w)), fmaxf(fmaxf(a1.x, a1.y), fmaxf(a1.z, a1.w))) * C2;
    MB = fmaxf(fmaxf(fmaxf(b0.x, b0.y), fmaxf(b0.z, b0.w)), fmaxf(fmaxf(b1.x, b1.y), fmaxf(b1.z, b1.w))) * C2;
  }
  unsigned pl[10], ph[10];
  {
    float la = 0.f, lb = 0.f;
#pragma unroll
    for (int u = 0; u < 5; ++u) {
      const float M = u < 4 ? MA : MB;
      const float p0 = __builtin_amdgcn_exp2f(fmaf(sc[u].x, C2, -M)), p1 = __builtin_amdgcn_exp2f(fmaf(sc[u].y, C2, -M));
      const float p2 = __builtin_amdgcn_exp2f(fmaf(sc[u].z, C2, -M)), p3 = __builtin_amdgcn_exp2f(fmaf(sc[u].w, C2, -M));
      const float t = (p0 + p1) + (p2 + p3);
      if (u < 4) la += t; else lb = t;
      pl[2 * u] = rho < 8 ? pk_bf16(p0, p1) : 0u; pl[2 * u + 1] = rho < 8 ? pk_bf16(p2, p3) : 0u;
      ph[2 * u] = __builtin_amdgcn_mov_dpp(pl[2 * u], ROR8, 0xf, 0xf, true);
      ph[2 * u + 1] = __builtin_amdgcn_mov_dpp(pl[2 * u + 1], ROR8, 0xf, 0xf, true);
      if (u == 2) PVX(XPT + 5);
    }
    la = x32_sum(x16_sum(la)); lb = x32_sum(x16_sum(lb));
    if (l < 8) {
      *(LAS float*)(L + L_LS + (l * 8 + w) * 4) = la;
      *(LAS float*)(L + L_LS + 256 + (l * 8 + w) * 4) = lb;
    }
  }
  lds_signal(L, S_LS, l);
  STAMP(13);
  PV_(XPT + 6);
  f32x4 acc[4];
#pragma unroll
  for (int q4 = 0; q4 < 4; ++q4) {
    const v4u a0 = {tr2(vf[0], vf[1], q4), tr2(vf[2], vf[3], q4), tr2(vf[4], vf[5], q4), tr2(vf[6], vf[7], q4)};
    const v4u a1 = {tr2(vf[0], vf[1], q4 + 4), tr2(vf[2], vf[3], q4 + 4), tr2(vf[4], vf[5], q4 + 4), tr2(vf[6], vf[7], q4 + 4)};
    acc[q4] = mfma32(a1, v4u{ph[0], ph[1], ph[2], ph[3]}, mfma32(a0, v4u{pl[0], pl[1], pl[2], pl[3]}, f32x4{0.f, 0.f, 0.f, 0.f}));
  }
#pragma unroll
  for (int n = XPT + 7; n < 20; ++n) PV_(n);
  const __amdgpu_buffer_rsrc_t rwo = MKRS(br ? woB : woA, 8 << 20);
  const int wop = (128 * h + 32 * (w >> 1) + 4 * (w & 1) + g) * 2048 + 512 * cq + rho * 16;
#define WLOAD(n) LDB(rwo, wop + ((n) >> 1) * (8 * 2048) + ((n) & 1) * 256, W_AUX)
#define PW(n) do { SB(); waitvm<WIN - 1>(); wo[n] = WLOAD(n); SB(); } while (0)
  SB(); wo[0] = WLOAD(0); SB();
  STAMP(14);
#pragma unroll
  for (int q4 = 0; q4 < 4; ++q4) {
    const v4u a0 = {tr2(vf[8], vf[9], q4), tr2(vf[10], vf[11], q4), tr2(vf[12], vf[13], q4), tr2(vf[14], vf[15], q4)};
    const v4u a1 = {tr2(vf[8], vf[9], q4 + 4), tr2(vf[10], vf[11], q4 + 4), tr2(vf[12], vf[13], q4 + 4), tr2(vf[14], vf[15], q4 + 4)};
    acc[q4] = mfma32(a1, v4u{ph[4], ph[5], ph[6], ph[7]}, mfma32(a0, v4u{pl[4], pl[5], pl[6], pl[7]}, acc[q4]));
  }
  LAS f32x4* dA = (LAS f32x4*)(L + L_RED + (w * 8 + (l & 7)) * 528 + g * 128 + (rho >> 3) * 64);
#pragma unroll
  for (int q4 = 0; q4 < 4; ++q4) dA[q4] = acc[q4];
  PW(1); PW(2); PW(3);
  STAMP(20);
#pragma unroll
  for (int q4 = 0; q4 < 4; ++q4) {
    const v2u a0 = {tr2(vf[16], vf[17], q4), tr2(vf[18], vf[19], q4)};
    const v2u a1 = {tr2(vf[16], vf[17], q4 + 4), tr2(vf[18], vf[19], q4 + 4)};
    dA[8 * 8 * 33 + q4] = mfma16(a1, v2u{ph[8], ph[9]}, mfma16(a0, v2u{pl[8], pl[9]}, f32x4{0.f, 0.f, 0.f, 0.f}));
  }
  SB(); wo[4] = WLOAD(4); SB();
  STAMP(6);
  lds_barrier();
  STAMP(15);
  {
    const int pb = tid >> 8, hd = (tid >> 5) & 7, quad = tid & 31;
    const LAS char* src = L + L_RED + pb * (8 * 8 * 528) + hd * 528 + quad * 16;
    f32x4 a = *(const LAS f32x4*)src;
#pragma unroll
    for (int ww = 1; ww < 8; ++ww) a += *(const LAS f32x4*)(src + ww * (8 * 528));
    const int rec = (pb * 32 + kvh * 8 + hd) * 64 + b * 16 + s;
    __builtin_amdgcn_raw_buffer_store_b128(nosent(a), rs, WS_PO + rec * 512 + quad * 16, 0, AUX_SC1);
  }
  STAMP(7);
  SB();
#pragma unroll
  for (int n = 5; n < 8; ++n) wo[n] = WLOAD(n);
  SB();
  v2u wt[16];
#pragma unroll
  for (int m = 0; m < 16; ++m) {
    wt[m] = v2u{tr2(wo[m >> 3], wo[2 + (m >> 3)], m & 7), tr2(wo[4 + (m >> 3)], wo[6 + (m >> 3)], m & 7)};
    asm volatile("" : "+v"(wt[m]));
  }
  const int sg = (l >> 2) & 3;
  STAMP(21);
  SB();

  {
    const int po = WS_PO + ((br * 32 + h) * 64 + (l & 3) * 16 + sg) * 512 + (16 * w + 4 * g) * 4;
    v4u pv[4];
#pragma unroll
    for (int n = 0; n < 4; ++n) pv[n] = __builtin_amdgcn_raw_buffer_load_b128(rs, po + n * 2048, 0, AUX_SC1);
    SB();
    lds_wait(L, S_W, 1);
    const f32x4 wg = *(const LAS f32x4*)(L + L_WGT + ((l & 3) * 16 + sg * 4) * 4);
    SB();
    for (unsigned spin = 0;; ++spin) {
      const bool bad = has_sent(pv[0]) | has_sent(pv[1]) | has_sent(pv[2]) | has_sent(pv[3]);
      if (__builtin_amdgcn_ballot_w64(bad) == 0ull || spin > SPIN_CAP) break;
      asm volatile("" ::: "memory");
      if (bad) {
#pragma unroll
        for (int n = 0; n < 4; ++n) pv[n] = __builtin_amdgcn_raw_buffer_load_b128(rs, po + n * 2048, 0, AUX_SC1);
      }
    }
    STAMP(8);
    f32x4 o = __builtin_bit_cast(f32x4, pv[0]) * wg.x + __builtin_bit_cast(f32x4, pv[1]) * wg.y +
              (__builtin_bit_cast(f32x4, pv[2]) * wg.z + __builtin_bit_cast(f32x4, pv[3]) * wg.w);
#pragma unroll
    for (int k = 0; k < 4; ++k) { float t = o[k]; t += dpp<ROR8>(t); t += dpp<ROR4>(t); o[k] = t; }
    const unsigned o01 = pk_bf16(o.x, o.y), o23 = pk_bf16(o.z, o.w);
    v2u bm[4];
#pragma unroll
    for (int k = 0; k < 4; ++k) bm[k] = v2u{sg == k ? o01 : 0u, sg == k ? o23 : 0u};
    LAS f32x4* d = (LAS f32x4*)(L + L_RED2 + (w * 4 + (l & 3)) * 1056 + g * 128 + sg * 16);
#pragma unroll
    for (int a = 0; a < 4; ++a) {
      f32x4 z = {0.f, 0.f, 0.f, 0.f};
#pragma unroll
      for (int k = 0; k < 4; ++k) z = mfma16(wt[4 * a + k], bm[k], z);
      d[(a >> 1) * 32 + (a & 1) * 4] = z;
    }
  }
  STAMP(18);
  lds_barrier();
  if (tid < 256) {
    const int bb = tid >> 6, quad = tid & 63;
    const LAS char* src = L + L_RED2 + bb * 1056 + quad * 16;
    f32x4 a = *(const LAS f32x4*)src;
#pragma unroll
    for (int ww = 1; ww < 8; ++ww) a += *(const LAS f32x4*)(src + ww * (4 * 1056));
    const int cons = (br * 4 + cq) * 32 + bb * 8 + (quad >> 3);
    __builtin_amdgcn_raw_buffer_store_b128(nosent(a), rs, WS_OUT + cons * 4096 + h * 128 + (quad & 7) * 16, 0, AUX_SC1);
  }
  STAMP(9);
  if (w == 7) {
    const int io = WS_OUT + idx * 4096 + l * 16;
    v4u pv[4];
    for (unsigned spin = 0;; ++spin) {
      asm volatile("" ::: "memory");
#pragma unroll
      for (int n = 0; n < 4; ++n) pv[n] = __builtin_amdgcn_raw_buffer_load_b128(rs, io + n * 1024, 0, AUX_SC1);
      const bool bad = has_sent(pv[0]) | has_sent(pv[1]) | has_sent(pv[2]) | has_sent(pv[3]);
      if (__builtin_amdgcn_ballot_w64(bad) == 0ull || spin > SPIN_CAP) break;
      __builtin_amdgcn_s_sleep(1);
    }
    f32x4 a = (__builtin_bit_cast(f32x4, pv[0]) + __builtin_bit_cast(f32x4, pv[1])) +
              (__builtin_bit_cast(f32x4, pv[2]) + __builtin_bit_cast(f32x4, pv[3]));
#pragma unroll
    for (int k = 0; k < 4; ++k) { float t = a[k]; t += dpp<ROR8>(t); a[k] = x32_sum(x16_sum(t)); }
    const int r = (l >> 3) & 3;
    const float v = r == 0 ? a.x : r == 1 ? a.y : r == 2 ? a.z : a.w;
    const int obr = idx >> 7, ocq = (idx >> 5) & 3, ob = (idx >> 3) & 3, oc = idx & 7;
    if (l < 32) out[ob * 2048 + obr * 1024 + ocq * 256 + oc * 32 + l] = (unsigned short)(pk_bf16(v, v) & 0xFFFFu);
    { const unsigned long long tgt = *(volatile LAS unsigned long long*)(L + 32); while (__builtin_amdgcn_s_memrealtime() < tgt) __builtin_amdgcn_s_sleep(SLP); }
  }
  STAMP(22);
  STAMP_FLUSH();
}

extern "C" void kernel_launch(void* const* d_in, const int* in_sizes, int n_in, void* d_out, int out_size, void* d_ws,
                              size_t ws_size, hipStream_t stream) {
  hipLaunchKernelGGL(fused_decode, dim3(256), dim3(576), 0, stream, (const char*)d_in[0], (const char*)d_in[1],
                     (const char*)d_in[2], (const char*)d_in[3], (const char*)d_in[4], (const char*)d_in[5],
                     (const char*)d_in[6], (const char*)d_in[7], (const char*)d_in[8], (const char*)d_in[9],
                     (unsigned short*)d_out, (char*)d_ws);
}
```

```cpp
#define DIAG 0
#include <hip/hip_runtime.h>
#include <stdint.h>

#define KV_AUX 2
#define W_AUX 2
#define WIN 4
#define XPT 8
#define PRETOUCH 1
#define STAMPS 0

typedef unsigned v4u __attribute__((ext_vector_type(4)));
typedef unsigned v2u __attribute__((ext_vector_type(2)));
typedef float f32x4 __attribute__((ext_vector_type(4)));
typedef __bf16 bf16x8 __attribute__((ext_vector_type(8)));
typedef short s16x4 __attribute__((ext_vector_type(4)));
#define GAS __attribute__((address_space(1)))
#define LAS __attribute__((address_space(3)))
#define SB() __builtin_amdgcn_sched_barrier(0)

constexpr int WS_EPOCH = 0;
constexpr int WS_FLAG = 4096;
constexpr int WS_QP = WS_FLAG + 256 * 128;
constexpr int WS_ML = WS_QP + 2 * 4 * 4 * 2 * 1024 * 4;
constexpr int WS_PO = WS_ML + 256 * 128;
constexpr int WS_OUT = WS_PO + 2 * 32 * 64 * 128 * 4;
constexpr int WS_END = WS_OUT + 256 * 32 * 32 * 4;
constexpr int WS_STAMP = WS_END;
constexpr int AUX_SC1 = 16;
constexpr unsigned SENT = 0xFFFFFFFFu;
constexpr unsigned SPIN_CAP = 1u << 16;

constexpr int L_SYNC = 0;
constexpr int L_WGT = 64;
constexpr int L_QRED = 512;
constexpr int L_QT = L_QRED + 16384;
constexpr int L_QS = L_QT + 1024;
constexpr int L_MX = L_QS + 2 * 8 * 272;
constexpr int L_LS = L_MX + 512;
constexpr int L_RED = L_LS + 512;
constexpr int L_RED2 = L_RED + 2 * 8 * 8 * 528;
constexpr int L_END = L_RED2 + 8 * 4 * 1056;
constexpr int S_QP = 0, S_Q = 4, S_MX = 8, S_LS = 12, S_W = 16;

#define MKRS(ptr, n) __builtin_amdgcn_make_buffer_rsrc((void*)(ptr), 0, (n), 0x00020000)
#define LDB(rs, off, aux) __builtin_amdgcn_raw_buffer_load_b128((rs), (off), 0, (aux))
__device__ __forceinline__ f32x4 mfma32(v4u a, v4u b, f32x4 c) {
  return __builtin_amdgcn_mfma_f32_16x16x32_bf16(__builtin_bit_cast(bf16x8, a), __builtin_bit_cast(bf16x8, b), c, 0, 0, 0);
}
__device__ __forceinline__ f32x4 mfma16(v2u a, v2u b, f32x4 c) {
  return __builtin_amdgcn_mfma_f32_16x16x16bf16_1k(__builtin_bit_cast(s16x4, a), __builtin_bit_cast(s16x4, b), c, 0, 0, 0);
}
__device__ __forceinline__ unsigned pk_bf16(float lo, float hi) {
  typedef __bf16 bf16x2 __attribute__((ext_vector_type(2)));
  typedef float f32x2 __attribute__((ext_vector_type(2)));
  f32x2 v = {lo, hi};
  return __builtin_bit_cast(unsigned, __builtin_convertvector(v, bf16x2));
}
__device__ __forceinline__ unsigned tr2(const v4u& r0, const v4u& r1, int dd) {
  return __builtin_amdgcn_perm(r1[dd >> 1], r0[dd >> 1], (dd & 1) ? 0x07060302u : 0x05040100u);
}
__device__ __forceinline__ float xg_max(float x) {
  auto s = __builtin_amdgcn_permlane16_swap(__float_as_uint(x), __float_as_uint(x), false, false);
  x = fmaxf(__uint_as_float(s[0]), __uint_as_float(s[1]));
  auto t = __builtin_amdgcn_permlane32_swap(__float_as_uint(x), __float_as_uint(x), false, false);
  return fmaxf(__uint_as_float(t[0]), __uint_as_float(t[1]));
}
__device__ __forceinline__ float x16_sum(float x) {
  auto s = __builtin_amdgcn_permlane16_swap(__float_as_uint(x), __float_as_uint(x), false, false);
  return __uint_as_float(s[0]) + __uint_as_float(s[1]);
}
__device__ __forceinline__ float x32_sum(float x) {
  auto t = __builtin_amdgcn_permlane32_swap(__float_as_uint(x), __float_as_uint(x), false, false);
  return __uint_as_float(t[0]) + __uint_as_float(t[1]);
}
template <int CTRL> __device__ __forceinline__ float dpp(float x) {
  return __builtin_bit_cast(float, __builtin_amdgcn_mov_dpp(__builtin_bit_cast(int, x), CTRL, 0xf, 0xf, true));
}
constexpr int ROR1 = 0x121, ROR2 = 0x122, ROR4 = 0x124, ROR8 = 0x128;
__device__ __forceinline__ v4u nosent(f32x4 v) {
  v4u u = __builtin_bit_cast(v4u, v);
  u.x = min(u.x, SENT - 1u); u.y = min(u.y, SENT - 1u); u.z = min(u.z, SENT - 1u); u.w = min(u.w, SENT - 1u);
  return u;
}
__device__ __forceinline__ bool has_sent(v4u u) { return max(max(u.x, u.y), max(u.z, u.w)) == SENT; }
__device__ __forceinline__ void lds_barrier() {
  asm volatile("s_waitcnt lgkmcnt(0)\n\ts_barrier" ::: "memory");
}
template <int N> __device__ __forceinline__ void waitvm() { asm volatile("s_waitcnt vmcnt(%0)" ::"i"(N) : "memory"); }
__device__ __forceinline__ void lds_signal(LAS char* L, int word, int l) {
  asm volatile("s_waitcnt lgkmcnt(0)" ::: "memory");
  if (l == 0) __hip_atomic_fetch_add((LAS unsigned*)(L + L_SYNC + word), 1u, __ATOMIC_RELAXED, __HIP_MEMORY_SCOPE_WORKGROUP);
  asm volatile("" ::: "memory");
}
__device__ __forceinline__ void lds_wait(LAS char* L, int word, unsigned want) {
  for (;;) {
    const unsigned v = __hip_atomic_load((LAS unsigned*)(L + L_SYNC + word), __ATOMIC_RELAXED, __HIP_MEMORY_SCOPE_WORKGROUP);
    if (__builtin_amdgcn_readfirstlane(v) >= want) break;
    __builtin_amdgcn_s_sleep(1);
  }
  asm volatile("" ::: "memory");
}

#define STAMP(n) do { if ((n) == 0 || (n) == DIAG) { SB(); ts[(n) == 0 ? 0 : 1] = __builtin_amdgcn_s_memrealtime(); SB(); } } while (0)
#define STAMP_FLUSH() do { if (w == 7) { const unsigned long long tgt = (DIAG ? ts[1] : ts[0]) + 4000ull; while (__builtin_amdgcn_s_memrealtime() < tgt) __builtin_amdgcn_s_sleep(2); } } while (0)

__global__ void __launch_bounds__(576, 3)
fused_decode(const char* __restrict__ xA, const char* __restrict__ kA, const char* __restrict__ vA, const char* __restrict__ wqA,
             const char* __restrict__ woA, const char* __restrict__ xB, const char* __restrict__ kB, const char* __restrict__ vB,
             const char* __restrict__ wqB, const char* __restrict__ woB, unsigned short* __restrict__ out, char* __restrict__ ws) {
  __shared__ __attribute__((aligned(16))) char smem[L_END];
  LAS char* L = (LAS char*)smem;
  const int c = blockIdx.x, tid = threadIdx.x, l = tid & 63, w = __builtin_amdgcn_readfirstlane(tid >> 6);
  const int idx = (c & 7) * 32 + (c >> 3), kvh = idx >> 6, gi = idx & 63;
  const int br = gi >> 5, cb = gi & 31;
  const int b = gi >> 4, s = gi & 15;
  const int h = kvh * 8 + ((gi >> 2) & 7), cq = gi & 3;
  const int g = l >> 4, rho = l & 15;
  const __amdgpu_buffer_rsrc_t rs = __builtin_amdgcn_make_buffer_rsrc((void*)ws, 0, WS_END, 0x00020000);
  unsigned long long ts[2] = {0, 0};
  STAMP(0);

  constexpr float C2 = 0.08838834764831845f * 1.4426950408889634f;

  if (w == 8) {
    __builtin_amdgcn_s_setprio(3);
    const unsigned long long epoch =
        __hip_atomic_load((GAS unsigned long long*)(ws + WS_EPOCH), __ATOMIC_RELAXED, __HIP_MEMORY_SCOPE_AGENT);
    if (l < 16) *(LAS unsigned*)(L + L_SYNC + l * 4) = 0u;
    unsigned touch = 0;
    {
      const size_t cub = (size_t)(b * 4 + kvh) * (8192 * 256) + (size_t)(128 * s) * 256;
      const int t = l & 15;
      const char* p = t < 4 ? kA + cub + t * (2048 * 256) : t < 8 ? vA + cub + (t - 4) * (2048 * 256)
                    : t == 8 ? kB + cub + 6144 * 256 : t == 9 ? vB + cub + 6144 * 256
                    : t < 14 ? (br ? woB : woA) + (size_t)(128 * h + 32 * (t - 10)) * 2048 + 512 * cq
                    : t == 14 ? (const char*)out : ws + WS_OUT + ((br * 4 + cq) * 32) * 4096;
      if (l < 16) touch = __builtin_nontemporal_load((const GAS unsigned*)p);
    }
    const int qslot = WS_QP + ((((br * 4 + kvh) * 4 + (l >> 4)) * 2 + (cb & 1)) * 1024 + (cb >> 1) * 64 + (l & 15) * 4) * 4;
    {
      const v4u sent = {SENT, SENT, SENT, SENT};
      __builtin_amdgcn_raw_buffer_store_b128(sent, rs, qslot, 0, AUX_SC1);
      const int po = WS_PO + ((br * 32 + h) * 64 + cq * 16) * 512 + l * 16;
#pragma unroll
      for (int n = 0; n < 8; ++n) __builtin_amdgcn_raw_buffer_store_b128(sent, rs, po + n * 1024, 0, AUX_SC1);
      const int oo = WS_OUT + idx * 4096 + l * 16;
#pragma unroll
      for (int n = 0; n < 4; ++n) __builtin_amdgcn_raw_buffer_store_b128(sent, rs, oo + n * 1024, 0, AUX_SC1);
      if (l < 8) __builtin_amdgcn_raw_buffer_store_b128(sent, rs, WS_ML + idx * 128 + l * 16, 0, AUX_SC1);
    }
    lds_barrier();
    const unsigned long long tag = epoch + 1ull;
    const unsigned tlo = (unsigned)tag, thi = (unsigned)(tag >> 32);
    waitvm<0>();
    asm volatile("" ::"v"(touch));
    if (l == 0) __builtin_amdgcn_raw_buffer_store_b64(v2u{tlo, thi}, rs, WS_FLAG + idx * 128, 0, AUX_SC1);
    STAMP(2);
    lds_wait(L, S_QP, 8);
    STAMP(1);
    {
      const LAS f32x4* src = (const LAS f32x4*)(L + L_QRED) + l;
      f32x4 a = src[0];
#pragma unroll
      for (int n = 1; n < 16; ++n) a += src[n * 64];
      LAS float* t = (LAS float*)(L + L_QT) + (l >> 3) * 32 + (l & 7);
      t[0] = a.x; t[8] = a.y; t[16] = a.z; t[24] = a.w;
      asm volatile("s_waitcnt lgkmcnt(0)" ::: "memory");
      a = *((const LAS f32x4*)(L + L_QT) + l);
      __builtin_amdgcn_raw_buffer_store_b128(nosent(a), rs, qslot, 0, AUX_SC1);
    }
    STAMP(16);
    {
      const int f1 = WS_FLAG + (kvh * 64 + l) * 128, f2 = WS_FLAG + ((br * 4 + cq) * 32 + (l & 31)) * 128;
      for (unsigned spin = 0;; ++spin) {
        asm volatile("" ::: "memory");
        const v2u x1 = __builtin_amdgcn_raw_buffer_load_b64(rs, f1, 0, AUX_SC1);
        const v2u x2 = __builtin_amdgcn_raw_buffer_load_b64(rs, f2, 0, AUX_SC1);
        if (__builtin_amdgcn_ballot_w64(((x1.x ^ tlo) | (x1.y ^ thi) | (x2.x ^ tlo) | (x2.y ^ thi)) != 0u) == 0ull || spin > SPIN_CAP) break;
        __builtin_amdgcn_s_sleep(8);
      }
      asm volatile("" ::: "memory");
    }
    STAMP(19);
    {
      const int qo = WS_QP + ((kvh * 4 + b) * 2 * 1024) * 4 + l * 16;
      v4u qg[16];
#pragma unroll
      for (int n = 0; n < 16; ++n) qg[n] = v4u{SENT, SENT, SENT, SENT};
      for (unsigned spin = 0;; ++spin) {
        asm volatile("" ::: "memory");
#pragma unroll
        for (int n = 0; n < 16; ++n)
          if (has_sent(qg[n])) qg[n] = __builtin_amdgcn_raw_buffer_load_b128(rs, qo + (n & 7) * 1024, (n >> 3) * (16 * 2 * 1024 * 4), AUX_SC1);
        bool bad = false;
#pragma unroll
        for (int n = 0; n < 16; ++n) bad |= has_sent(qg[n]);
        if (__builtin_amdgcn_ballot_w64(bad) == 0ull || spin > SPIN_CAP) break;
        __builtin_amdgcn_s_sleep(4);
      }
#pragma unroll
      for (int n = 0; n < 8; ++n) {
        const f32x4 q4 = __builtin_bit_cast(f32x4, qg[(n >> 2) * 8 + (n & 3)]) + __builtin_bit_cast(f32x4, qg[(n >> 2) * 8 + 4 + (n & 3)]);
        *(LAS v2u*)(L + L_QS + (2 * n + (l >> 5)) * 272 + (l & 31) * 8) = v2u{pk_bf16(q4.x, q4.y), pk_bf16(q4.z, q4.w)};
      }
    }
    lds_signal(L, S_Q, l);
    STAMP(3);
    if (idx == 0) {
      for (int pass = 0; pass < 4; ++pass)
        for (unsigned spin = 0;; ++spin) {
          asm volatile("" ::: "memory");
          const v2u x2 = __builtin_amdgcn_raw_buffer_load_b64(rs, WS_FLAG + (pass * 64 + l) * 128, 0, AUX_SC1);
          if (__builtin_amdgcn_ballot_w64(((x2.x ^ tlo) | (x2.y ^ thi)) != 0u) == 0ull || spin > SPIN_CAP) break;
          __builtin_amdgcn_s_sleep(8);
        }
      if (l == 0) __hip_atomic_store((GAS unsigned long long*)(ws + WS_EPOCH), tag, __ATOMIC_RELAXED, __HIP_MEMORY_SCOPE_AGENT);
    }
    lds_wait(L, S_LS, 8);
    if (l < 16) {
      const LAS f32x4* ls = (const LAS f32x4*)(L + L_LS + l * 32);
      const LAS f32x4* mx = (const LAS f32x4*)(L + L_MX + l * 32);
      const f32x4 l0 = ls[0], l1 = ls[1], m0 = mx[0], m1 = mx[1];
      const float lsum = ((l0.x + l0.y) + (l0.z + l0.w)) + ((l1.x + l1.y) + (l1.z + l1.w));
      const float mm = fmaxf(fmaxf(fmaxf(m0.x, m0.y), fmaxf(m0.z, m0.w)), fmaxf(fmaxf(m1.x, m1.y), fmaxf(m1.z, m1.w))) * C2;
      __builtin_amdgcn_raw_buffer_store_b64(v2u{min(__float_as_uint(mm), SENT - 1u), min(__float_as_uint(lsum), SENT - 1u)}, rs,
                                            WS_ML + idx * 128 + l * 8, 0, AUX_SC1);
    }
    STAMP(5);
    lds_barrier();
    {
      const int mo = WS_ML + (kvh * 64 + l) * 128 + (br * 8 + (h & 7)) * 8;
      v2u ml = {SENT, SENT};
      for (unsigned spin = 0;; ++spin) {
        asm volatile("" ::: "memory");
        if (max(ml.x, ml.y) == SENT) ml = __builtin_amdgcn_raw_buffer_load_b64(rs, mo, 0, AUX_SC1);
        if (__builtin_amdgcn_ballot_w64(max(ml.x, ml.y) == SENT) == 0ull || spin > SPIN_CAP) break;
        __builtin_amdgcn_s_sleep(4);
      }
      const float m = __uint_as_float(ml.x), ls = __uint_as_float(ml.y);
      float M = m;
      M = fmaxf(M, dpp<ROR8>(M)); M = fmaxf(M, dpp<ROR4>(M)); M = fmaxf(M, dpp<ROR2>(M)); M = fmaxf(M, dpp<ROR1>(M));
      const float e = __builtin_amdgcn_exp2f(m - M);
      float Ls = e * ls;
      Ls += dpp<ROR8>(Ls); Ls += dpp<ROR4>(Ls); Ls += dpp<ROR2>(Ls); Ls += dpp<ROR1>(Ls);
      *(LAS float*)(L + L_WGT + ((l >> 4) * 16 + (l & 3) * 4 + ((l >> 2) & 3)) * 4) = e / Ls;
    }
    lds_signal(L, S_W, l);
    STAMP(12);
    STAMP(22);
    STAMP_FLUSH();
    return;
  }

  const int qrow = 512 * (cb & 1) + 64 * w;
  const v4u xf = *(const GAS v4u*)((br ? xB : xA) + ((l & 3) * 1024 + qrow + 16 * g + 8 * ((l >> 3) & 1)) * 2);
  v4u wr[8];
  const __amdgpu_buffer_rsrc_t rwq = MKRS(br ? wqB : wqA, 8 << 20);
  const int wqp = (qrow + 8 * (l >> 3)) * 8192 + (kvh * 1024 + (cb >> 1) * 64) * 2 + (l & 7) * 16;
#pragma unroll
  for (int n = 0; n < WIN; ++n) wr[n] = LDB(rwq, wqp + n * 8192, W_AUX);
  SB();
  asm volatile("s_barrier" ::: "memory");
  SB();
#pragma unroll
  for (int n = WIN; n < 8; ++n) { waitvm<WIN - 1>(); wr[n] = LDB(rwq, wqp + n * 8192, W_AUX); SB(); }
  const int wbase = (b * 4 + kvh) * (8192 * 256) + (128 * s + 16 * w) * 256;
  const int koff = wbase + (4 * (rho & 3) + ((rho >> 2) & 1)) * 256 + g * 16 + 64 * (rho >> 3);
  const int voff = wbase + l * 16;
  constexpr int TS = 2048 * 256;
  v4u kf[20], vf[20], wo[8];
  const __amdgpu_buffer_rsrc_t rkA = MKRS(kA, 32 << 20), rkB = MKRS(kB, 32 << 20), rvA = MKRS(vA, 32 << 20), rvB = MKRS(vB, 32 << 20);
#define KLOAD(n) __builtin_amdgcn_raw_buffer_load_b128((n) < 16 ? rkA : rkB, koff + ((n) & 1) * 128 + (((n) >> 1) & 1) * 512, ((n) < 16 ? ((n) >> 2) : 3) * TS, KV_AUX)
#define VLOAD(n) __builtin_amdgcn_raw_buffer_load_b128((n) < 16 ? rvA : rvB, voff + ((n) & 3) * 1024, ((n) < 16 ? ((n) >> 2) : 3) * TS, KV_AUX)
#define PK(n) do { SB(); waitvm<WIN - 1>(); kf[n] = KLOAD(n); SB(); } while (0)
#define PV_(n) do { SB(); waitvm<WIN - 1>(); vf[n] = VLOAD(n); SB(); } while (0)
#define PVX(n) do { SB(); vf[n] = VLOAD(n); SB(); } while (0)
#pragma unroll
  for (int n = 0; n < WIN; ++n) PK(n);
  SB(); kf[WIN] = KLOAD(WIN); SB();

  {
    f32x4 qa[8];
#pragma unroll
    for (int dd = 0; dd < 8; ++dd) {
      const v4u a = {tr2(wr[0], wr[1], dd), tr2(wr[2], wr[3], dd), tr2(wr[4], wr[5], dd), tr2(wr[6], wr[7], dd)};
      qa[dd] = mfma32(a, xf, f32x4{0.f, 0.f, 0.f, 0.f});
    }
    if ((l & 12) == ((g >> 1) << 3)) {
      LAS f32x4* d = (LAS f32x4*)(L + L_QRED + ((w * 2 + (g >> 1)) * 4 + (l & 3)) * 256 + (g & 1) * 128);
#pragma unroll
      for (int dd = 0; dd < 8; ++dd) d[dd] = qa[dd];
    }
  }
  lds_signal(L, S_QP, l);
  STAMP(1);
#pragma unroll
  for (int n = WIN + 1; n < 20; ++n) PK(n);
#pragma unroll
  for (int n = 0; n < XPT; ++n) PV_(n);
  STAMP(4);
  lds_wait(L, S_Q, 1);
  STAMP(11);
  PVX(XPT);

  f32x4 sc[5];
  {
    const LAS char* qp = L + L_QS + (l & 7) * 272 + g * 16 + 64 * (rho >> 3);
    const v4u qa0 = *(const LAS v4u*)qp, qa1 = *(const LAS v4u*)(qp + 128);
    const v4u qb0 = *(const LAS v4u*)(qp + 8 * 272), qb1 = *(const LAS v4u*)(qp + 8 * 272 + 128);
#pragma unroll
    for (int u = 0; u < 5; ++u) {
      const f32x4 z = {0.f, 0.f, 0.f, 0.f};
      const f32x4 t1 = mfma32(kf[4 * u + 1], u < 4 ? qa1 : qb1, mfma32(kf[4 * u], u < 4 ? qa0 : qb0, z));
      const f32x4 t2 = mfma32(kf[4 * u + 3], u < 4 ? qa1 : qb1, mfma32(kf[4 * u + 2], u < 4 ? qa0 : qb0, z));
#pragma unroll
      for (int r = 0; r < 4; ++r) {
        auto sw = __builtin_amdgcn_permlane32_swap(__float_as_uint(t1[r]), __float_as_uint(t2[r]), false, false);
        sc[u][r] = __uint_as_float(sw[0]) + dpp<ROR8>(__uint_as_float(sw[1]));
      }
      if (u == 2) PVX(XPT + 1);
    }
  }
  {
    float mB = fmaxf(fmaxf(sc[4].x, sc[4].y), fmaxf(sc[4].z, sc[4].w));
    float mA = fmaxf(fmaxf(sc[0].x, sc[0].y), fmaxf(sc[0].z, sc[0].w));
#pragma unroll
    for (int u = 1; u < 4; ++u) mA = fmaxf(mA, fmaxf(fmaxf(sc[u].x, sc[u].y), fmaxf(sc[u].z, sc[u].w)));
    mA = xg_max(mA); mB = xg_max(mB);
    if (l < 8) {
      *(LAS float*)(L + L_MX + (l * 8 + w) * 4) = mA;
      *(LAS float*)(L + L_MX + 256 + (l * 8 + w) * 4) = mB;
    }
  }
  lds_signal(L, S_MX, l);
  STAMP(5);
  PV_(XPT + 2);
  PV_(XPT + 3);
  lds_wait(L, S_MX, 8);
  STAMP(12);
  PVX(XPT + 4);
  float MA, MB;
  {
    const LAS f32x4* m = (const LAS f32x4*)(L + L_MX + (l & 7) * 32);
    const f32x4 a0 = m[0], a1 = m[1], b0 = m[16], b1 = m[17];
    MA = fmaxf(fmaxf(fmaxf(a0.x, a0.y), fmaxf(a0.z, a0.w)), fmaxf(fmaxf(a1.x, a1.y), fmaxf(a1.z, a1.w))) * C2;
    MB = fmaxf(fmaxf(fmaxf(b0.x, b0.y), fmaxf(b0.z, b0.w)), fmaxf(fmaxf(b1.x, b1.y), fmaxf(b1.z, b1.w))) * C2;
  }
  unsigned pl[10], ph[10];
  {
    float la = 0.f, lb = 0.f;
#pragma unroll
    for (int u = 0; u < 5; ++u) {
      const float M = u < 4 ? MA : MB;
      const float p0 = __builtin_amdgcn_exp2f(fmaf(sc[u].x, C2, -M)), p1 = __builtin_amdgcn_exp2f(fmaf(sc[u].y, C2, -M));
      const float p2 = __builtin_amdgcn_exp2f(fmaf(sc[u].z, C2, -M)), p3 = __builtin_amdgcn_exp2f(fmaf(sc[u].w, C2, -M));
      const float t = (p0 + p1) + (p2 + p3);
      if (u < 4) la += t; else lb = t;
      pl[2 * u] = rho < 8 ? pk_bf16(p0, p1) : 0u; pl[2 * u + 1] = rho < 8 ? pk_bf16(p2, p3) : 0u;
      ph[2 * u] = __builtin_amdgcn_mov_dpp(pl[2 * u], ROR8, 0xf, 0xf, true);
      ph[2 * u + 1] = __builtin_amdgcn_mov_dpp(pl[2 * u + 1], ROR8, 0xf, 0xf, true);
      if (u == 2) PVX(XPT + 5);
    }
    la = x32_sum(x16_sum(la)); lb = x32_sum(x16_sum(lb));
    if (l < 8) {
      *(LAS float*)(L + L_LS + (l * 8 + w) * 4) = la;
      *(LAS float*)(L + L_LS + 256 + (l * 8 + w) * 4) = lb;
    }
  }
  lds_signal(L, S_LS, l);
  STAMP(13);
  PV_(XPT + 6);
  f32x4 acc[4];
#pragma unroll
  for (int q4 = 0; q4 < 4; ++q4) {
    const v4u a0 = {tr2(vf[0], vf[1], q4), tr2(vf[2], vf[3], q4), tr2(vf[4], vf[5], q4), tr2(vf[6], vf[7], q4)};
    const v4u a1 = {tr2(vf[0], vf[1], q4 + 4), tr2(vf[2], vf[3], q4 + 4), tr2(vf[4], vf[5], q4 + 4), tr2(vf[6], vf[7], q4 + 4)};
    acc[q4] = mfma32(a1, v4u{ph[0], ph[1], ph[2], ph[3]}, mfma32(a0, v4u{pl[0], pl[1], pl[2], pl[3]}, f32x4{0.f, 0.f, 0.f, 0.f}));
  }
#pragma unroll
  for (int n = XPT + 7; n < 20; ++n) PV_(n);
  const __amdgpu_buffer_rsrc_t rwo = MKRS(br ? woB : woA, 8 << 20);
  const int wop = (128 * h + 32 * (w >> 1) + 4 * (w & 1) + g) * 2048 + 512 * cq + rho * 16;
#define WLOAD(n) LDB(rwo, wop + ((n) >> 1) * (8 * 2048) + ((n) & 1) * 256, W_AUX)
#define PW(n) do { SB(); waitvm<WIN - 1>(); wo[n] = WLOAD(n); SB(); } while (0)
  SB(); wo[0] = WLOAD(0); SB();
  STAMP(14);
#pragma unroll
  for (int q4 = 0; q4 < 4; ++q4) {
    const v4u a0 = {tr2(vf[8], vf[9], q4), tr2(vf[10], vf[11], q4), tr2(vf[12], vf[13], q4), tr2(vf[14], vf[15], q4)};
    const v4u a1 = {tr2(vf[8], vf[9], q4 + 4), tr2(vf[10], vf[11], q4 + 4), tr2(vf[12], vf[13], q4 + 4), tr2(vf[14], vf[15], q4 + 4)};
    acc[q4] = mfma32(a1, v4u{ph[4], ph[5], ph[6], ph[7]}, mfma32(a0, v4u{pl[4], pl[5], pl[6], pl[7]}, acc[q4]));
  }
  LAS f32x4* dA = (LAS f32x4*)(L + L_RED + (w * 8 + (l & 7)) * 528 + g * 128 + (rho >> 3) * 64);
#pragma unroll
  for (int q4 = 0; q4 < 4; ++q4) dA[q4] = acc[q4];
  PW(1); PW(2); PW(3);
  STAMP(20);
#pragma unroll
  for (int q4 = 0; q4 < 4; ++q4) {
    const v2u a0 = {tr2(vf[16], vf[17], q4), tr2(vf[18], vf[19], q4)};
    const v2u a1 = {tr2(vf[16], vf[17], q4 + 4), tr2(vf[18], vf[19], q4 + 4)};
    dA[8 * 8 * 33 + q4] = mfma16(a1, v2u{ph[8], ph[9]}, mfma16(a0, v2u{pl[8], pl[9]}, f32x4{0.f, 0.f, 0.f, 0.f}));
  }
  SB(); wo[4] = WLOAD(4); SB();
  STAMP(6);
  lds_barrier();
  STAMP(15);
  {
    const int pb = tid >> 8, hd = (tid >> 5) & 7, quad = tid & 31;
    const LAS char* src = L + L_RED + pb * (8 * 8 * 528) + hd * 528 + quad * 16;
    f32x4 a = *(const LAS f32x4*)src;
#pragma unroll
    for (int ww = 1; ww < 8; ++ww) a += *(const LAS f32x4*)(src + ww * (8 * 528));
    const int rec = (pb * 32 + kvh * 8 + hd) * 64 + b * 16 + s;
    __builtin_amdgcn_raw_buffer_store_b128(nosent(a), rs, WS_PO + rec * 512 + quad * 16, 0, AUX_SC1);
  }
  STAMP(7);
  SB();
#pragma unroll
  for (int n = 5; n < 8; ++n) wo[n] = WLOAD(n);
  SB();
  v2u wt[16];
#pragma unroll
  for (int m = 0; m < 16; ++m) {
    wt[m] = v2u{tr2(wo[m >> 3], wo[2 + (m >> 3)], m & 7), tr2(wo[4 + (m >> 3)], wo[6 + (m >> 3)], m & 7)};
    asm volatile("" : "+v"(wt[m]));
  }
  const int sg = (l >> 2) & 3;
  STAMP(21);
  SB();

  {
    const int po = WS_PO + ((br * 32 + h) * 64 + (l & 3) * 16 + sg) * 512 + (16 * w + 4 * g) * 4;
    v4u pv[4];
#pragma unroll
    for (int n = 0; n < 4; ++n) pv[n] = __builtin_amdgcn_raw_buffer_load_b128(rs, po + n * 2048, 0, AUX_SC1);
    SB();
    lds_wait(L, S_W, 1);
    const f32x4 wg = *(const LAS f32x4*)(L + L_WGT + ((l & 3) * 16 + sg * 4) * 4);
    SB();
    for (unsigned spin = 0;; ++spin) {
      const bool bad = has_sent(pv[0]) | has_sent(pv[1]) | has_sent(pv[2]) | has_sent(pv[3]);
      if (__builtin_amdgcn_ballot_w64(bad) == 0ull || spin > SPIN_CAP) break;
      asm volatile("" ::: "memory");
      if (bad) {
#pragma unroll
        for (int n = 0; n < 4; ++n) pv[n] = __builtin_amdgcn_raw_buffer_load_b128(rs, po + n * 2048, 0, AUX_SC1);
      }
    }
    STAMP(8);
    f32x4 o = __builtin_bit_cast(f32x4, pv[0]) * wg.x + __builtin_bit_cast(f32x4, pv[1]) * wg.y +
              (__builtin_bit_cast(f32x4, pv[2]) * wg.z + __builtin_bit_cast(f32x4, pv[3]) * wg.w);
#pragma unroll
    for (int k = 0; k < 4; ++k) { float t = o[k]; t += dpp<ROR8>(t); t += dpp<ROR4>(t); o[k] = t; }
    const unsigned o01 = pk_bf16(o.x, o.y), o23 = pk_bf16(o.z, o.w);
    v2u bm[4];
#pragma unroll
    for (int k = 0; k < 4; ++k) bm[k] = v2u{sg == k ? o01 : 0u, sg == k ? o23 : 0u};
    LAS f32x4* d = (LAS f32x4*)(L + L_RED2 + (w * 4 + (l & 3)) * 1056 + g * 128 + sg * 16);
#pragma unroll
    for (int a = 0; a < 4; ++a) {
      f32x4 z = {0.f, 0.f, 0.f, 0.f};
#pragma unroll
      for (int k = 0; k < 4; ++k) z = mfma16(wt[4 * a + k], bm[k], z);
      d[(a >> 1) * 32 + (a & 1) * 4] = z;
    }
  }
  STAMP(18);
  lds_barrier();
  if (tid < 256) {
    const int bb = tid >> 6, quad = tid & 63;
    const LAS char* src = L + L_RED2 + bb * 1056 + quad * 16;
    f32x4 a = *(const LAS f32x4*)src;
#pragma unroll
    for (int ww = 1; ww < 8; ++ww) a += *(const LAS f32x4*)(src + ww * (4 * 1056));
    const int cons = (br * 4 + cq) * 32 + bb * 8 + (quad >> 3);
    __builtin_amdgcn_raw_buffer_store_b128(nosent(a), rs, WS_OUT + cons * 4096 + h * 128 + (quad & 7) * 16, 0, AUX_SC1);
  }
  STAMP(9);
  if (w == 7) {
    const int io = WS_OUT + idx * 4096 + l * 16;
    v4u pv[4];
    for (unsigned spin = 0;; ++spin) {
      asm volatile("" ::: "memory");
#pragma unroll
      for (int n = 0; n < 4; ++n) pv[n] = __builtin_amdgcn_raw_buffer_load_b128(rs, io + n * 1024, 0, AUX_SC1);
      const bool bad = has_sent(pv[0]) | has_sent(pv[1]) | has_sent(pv[2]) | has_sent(pv[3]);
      if (__builtin_amdgcn_ballot_w64(bad) == 0ull || spin > SPIN_CAP) break;
      __builtin_amdgcn_s_sleep(1);
    }
    f32x4 a = (__builtin_bit_cast(f32x4, pv[0]) + __builtin_bit_cast(f32x4, pv[1])) +
              (__builtin_bit_cast(f32x4, pv[2]) + __builtin_bit_cast(f32x4, pv[3]));
#pragma unroll
    for (int k = 0; k < 4; ++k) { float t = a[k]; t += dpp<ROR8>(t); a[k] = x32_sum(x16_sum(t)); }
    const int r = (l >> 3) & 3;
    const float v = r == 0 ? a.x : r == 1 ? a.y : r == 2 ? a.z : a.w;
    const int obr = idx >> 7, ocq = (idx >> 5) & 3, ob = (idx >> 3) & 3, oc = idx & 7;
    if (l < 32) out[ob * 2048 + obr * 1024 + ocq * 256 + oc * 32 + l] = (unsigned short)(pk_bf16(v, v) & 0xFFFFu);
    STAMP(10);
  }
  STAMP(22);
  STAMP_FLUSH();
}

__global__ void __launch_bounds__(64) k_pre(char* ws) {
  const unsigned long long t0 = __builtin_amdgcn_s_memrealtime();
  if (threadIdx.x == 0 && blockIdx.x == 0) *(GAS unsigned long long*)(ws + 2048) = t0;
}
__global__ void __launch_bounds__(64) k_post(char* ws) {
  const unsigned long long tgt = *(GAS unsigned long long*)(ws + 2048) + 6000ull;
  while (__builtin_amdgcn_s_memrealtime() < tgt) __builtin_amdgcn_s_sleep(2);
}
extern "C" void kernel_launch(void* const* d_in, const int* in_sizes, int n_in, void* d_out, int out_size, void* d_ws,
                              size_t ws_size, hipStream_t stream) {
  hipLaunchKernelGGL(k_pre, dim3(1), dim3(64), 0, stream, (char*)d_ws);
  hipLaunchKernelGGL(fused_decode, dim3(256), dim3(576), 0, stream, (const char*)d_in[0], (const char*)d_in[1],
                     (const char*)d_in[2], (const char*)d_in[3], (const char*)d_in[4], (const char*)d_in[5],
                     (const char*)d_in[6], (const char*)d_in[7], (const char*)d_in[8], (const char*)d_in[9],
                     (unsigned short*)d_out, (char*)d_ws);
}
```

```cpp
#define DIAG 0
#include <hip/hip_runtime.h>
#include <stdint.h>

#define KV_AUX 2
#define W_AUX 2
#define WIN 4
#define XPT 8
#define PRETOUCH 1
#define STAMPS 0

typedef unsigned v4u __attribute__((ext_vector_type(4)));
typedef unsigned v2u __attribute__((ext_vector_type(2)));
typedef float f32x4 __attribute__((ext_vector_type(4)));
typedef __bf16 bf16x8 __attribute__((ext_vector_type(8)));
typedef short s16x4 __attribute__((ext_vector_type(4)));
#define GAS __attribute__((address_space(1)))
#define LAS __attribute__((address_space(3)))
#define SB() __builtin_amdgcn_sched_barrier(0)

constexpr int WS_EPOCH = 0;
constexpr int WS_FLAG = 4096;
constexpr int WS_QP = WS_FLAG + 256 * 128;
constexpr int WS_ML = WS_QP + 2 * 4 * 4 * 2 * 1024 * 4;
constexpr int WS_PO = WS_ML + 256 * 128;
constexpr int WS_OUT = WS_PO + 2 * 32 * 64 * 128 * 4;
constexpr int WS_END = WS_OUT + 256 * 32 * 32 * 4;
constexpr int WS_STAMP = WS_END;
constexpr int AUX_SC1 = 16;
constexpr unsigned SENT = 0xFFFFFFFFu;
constexpr unsigned SPIN_CAP = 1u << 16;

constexpr int L_SYNC = 0;
constexpr int L_WGT = 64;
constexpr int L_QRED = 512;
constexpr int L_QT = L_QRED + 16384;
constexpr int L_QS = L_QT + 1024;
constexpr int L_MX = L_QS + 2 * 8 * 272;
constexpr int L_LS = L_MX + 512;
constexpr int L_RED = L_LS + 512;
constexpr int L_RED2 = L_RED + 2 * 8 * 8 * 528;
constexpr int L_END = L_RED2 + 8 * 4 * 1056;
constexpr int S_QP = 0, S_Q = 4, S_MX = 8, S_LS = 12, S_W = 16;

#define MKRS(ptr, n) __builtin_amdgcn_make_buffer_rsrc((void*)(ptr), 0, (n), 0x00020000)
#define LDB(rs, off, aux) __builtin_amdgcn_raw_buffer_load_b128((rs), (off), 0, (aux))
__device__ __forceinline__ f32x4 mfma32(v4u a, v4u b, f32x4 c) {
  return __builtin_amdgcn_mfma_f32_16x16x32_bf16(__builtin_bit_cast(bf16x8, a), __builtin_bit_cast(bf16x8, b), c, 0, 0, 0);
}
__device__ __forceinline__ f32x4 mfma16(v2u a, v2u b, f32x4 c) {
  return __builtin_amdgcn_mfma_f32_16x16x16bf16_1k(__builtin_bit_cast(s16x4, a), __builtin_bit_cast(s16x4, b), c, 0, 0, 0);
}
__device__ __forceinline__ unsigned pk_bf16(float lo, float hi) {
  typedef __bf16 bf16x2 __attribute__((ext_vector_type(2)));
  typedef float f32x2 __attribute__((ext_vector_type(2)));
  f32x2 v = {lo, hi};
  return __builtin_bit_cast(unsigned, __builtin_convertvector(v, bf16x2));
}
__device__ __forceinline__ unsigned tr2(const v4u& r0, const v4u& r1, int dd) {
  return __builtin_amdgcn_perm(r1[dd >> 1], r0[dd >> 1], (dd & 1) ? 0x07060302u : 0x05040100u);
}
__device__ __forceinline__ float xg_max(float x) {
  auto s = __builtin_amdgcn_permlane16_swap(__float_as_uint(x), __float_as_uint(x), false, false);
  x = fmaxf(__uint_as_float(s[0]), __uint_as_float(s[1]));
  auto t = __builtin_amdgcn_permlane32_swap(__float_as_uint(x), __float_as_uint(x), false, false);
  return fmaxf(__uint_as_float(t[0]), __uint_as_float(t[1]));
}
__device__ __forceinline__ float x16_sum(float x) {
  auto s = __builtin_amdgcn_permlane16_swap(__float_as_uint(x), __float_as_uint(x), false, false);
  return __uint_as_float(s[0]) + __uint_as_float(s[1]);
}
__device__ __forceinline__ float x32_sum(float x) {
  auto t = __builtin_amdgcn_permlane32_swap(__float_as_uint(x), __float_as_uint(x), false, false);
  return __uint_as_float(t[0]) + __uint_as_float(t[1]);
}
template <int CTRL> __device__ __forceinline__ float dpp(float x) {
  return __builtin_bit_cast(float, __builtin_amdgcn_mov_dpp(__builtin_bit_cast(int, x), CTRL, 0xf, 0xf, true));
}
constexpr int ROR1 = 0x121, ROR2 = 0x122, ROR4 = 0x124, ROR8 = 0x128;
__device__ __forceinline__ v4u nosent(f32x4 v) {
  v4u u = __builtin_bit_cast(v4u, v);
  u.x = min(u.x, SENT - 1u); u.y = min(u.y, SENT - 1u); u.z = min(u.z, SENT - 1u); u.w = min(u.w, SENT - 1u);
  return u;
}
__device__ __forceinline__ bool has_sent(v4u u) { return max(max(u.x, u.y), max(u.z, u.w)) == SENT; }
__device__ __forceinline__ void lds_barrier() {
  asm volatile("s_waitcnt lgkmcnt(0)\n\ts_barrier" ::: "memory");
}
template <int N> __device__ __forceinline__ void waitvm() { asm volatile("s_waitcnt vmcnt(%0)" ::"i"(N) : "memory"); }
__device__ __forceinline__ void lds_signal(LAS char* L, int word, int l) {
  asm volatile("s_waitcnt lgkmcnt(0)" ::: "memory");
  if (l == 0) __hip_atomic_fetch_add((LAS unsigned*)(L + L_SYNC + word), 1u, __ATOMIC_RELAXED, __HIP_MEMORY_SCOPE_WORKGROUP);
  asm volatile("" ::: "memory");
}
__device__ __forceinline__ void lds_wait(LAS char* L, int word, unsigned want) {
  for (;;) {
    const unsigned v = __hip_atomic_load((LAS unsigned*)(L + L_SYNC + word), __ATOMIC_RELAXED, __HIP_MEMORY_SCOPE_WORKGROUP);
    if (__builtin_amdgcn_readfirstlane(v) >= want) break;
    __builtin_amdgcn_s_sleep(1);
  }
  asm volatile("" ::: "memory");
}

#define STAMP(n) do { if ((n) == 0 || (n) == DIAG) { SB(); ts[(n) == 0 ? 0 : 1] = __builtin_amdgcn_s_memrealtime(); SB(); } } while (0)
#define STAMP_FLUSH() do { if (w == 7) { const unsigned long long tgt = (DIAG ? ts[1] : ts[0]) + 4000ull; while (__builtin_amdgcn_s_memrealtime() < tgt) __builtin_amdgcn_s_sleep(2); } } while (0)

__global__ void __launch_bounds__(576, 3)
fused_decode(const char* __restrict__ xA, const char* __restrict__ kA, const char* __restrict__ vA, const char* __restrict__ wqA,
             const char* __restrict__ woA, const char* __restrict__ xB, const char* __restrict__ kB, const char* __restrict__ vB,
             const char* __restrict__ wqB, const char* __restrict__ woB, unsigned short* __restrict__ out, char* __restrict__ ws) {
  __shared__ __attribute__((aligned(16))) char smem[L_END];
  LAS char* L = (LAS char*)smem;
  const int c = blockIdx.x, tid = threadIdx.x, l = tid & 63, w = __builtin_amdgcn_readfirstlane(tid >> 6);
  const int idx = (c & 7) * 32 + (c >> 3), kvh = idx >> 6, gi = idx & 63;
  const int br = gi >> 5, cb = gi & 31;
  const int b = gi >> 4, s = gi & 15;
  const int h = kvh * 8 + ((gi >> 2) & 7), cq = gi & 3;
  const int g = l >> 4, rho = l & 15;
  const __amdgpu_buffer_rsrc_t rs = __builtin_amdgcn_make_buffer_rsrc((void*)ws, 0, WS_END, 0x00020000);
  unsigned long long ts[2] = {0, 0};
  STAMP(0);

  constexpr float C2 = 0.08838834764831845f * 1.4426950408889634f;

  if (w == 8) {
    __builtin_amdgcn_s_setprio(3);
    const unsigned long long epoch =
        __hip_atomic_load((GAS unsigned long long*)(ws + WS_EPOCH), __ATOMIC_RELAXED, __HIP_MEMORY_SCOPE_AGENT);
    if (l < 16) *(LAS unsigned*)(L + L_SYNC + l * 4) = 0u;
    unsigned touch = 0;
    {
      const size_t cub = (size_t)(b * 4 + kvh) * (8192 * 256) + (size_t)(128 * s) * 256;
      const int t = l & 15;
      const char* p = t < 4 ? kA + cub + t * (2048 * 256) : t < 8 ? vA + cub + (t - 4) * (2048 * 256)
                    : t == 8 ? kB + cub + 6144 * 256 : t == 9 ? vB + cub + 6144 * 256
                    : t < 14 ? (br ? woB : woA) + (size_t)(128 * h + 32 * (t - 10)) * 2048 + 512 * cq
                    : t == 14 ? (const char*)out : ws + WS_OUT + ((br * 4 + cq) * 32) * 4096;
      if (l < 16) touch = __builtin_nontemporal_load((const GAS unsigned*)p);
    }
    const int qslot = WS_QP + ((((br * 4 + kvh) * 4 + (l >> 4)) * 2 + (cb & 1)) * 1024 + (cb >> 1) * 64 + (l & 15) * 4) * 4;
    {
      const v4u sent = {SENT, SENT, SENT, SENT};
      __builtin_amdgcn_raw_buffer_store_b128(sent, rs, qslot, 0, AUX_SC1);
      const int po = WS_PO + ((br * 32 + h) * 64 + cq * 16) * 512 + l * 16;
#pragma unroll
      for (int n = 0; n < 8; ++n) __builtin_amdgcn_raw_buffer_store_b128(sent, rs, po + n * 1024, 0, AUX_SC1);
      const int oo = WS_OUT + idx * 4096 + l * 16;
#pragma unroll
      for (int n = 0; n < 4; ++n) __builtin_amdgcn_raw_buffer_store_b128(sent, rs, oo + n * 1024, 0, AUX_SC1);
      if (l < 8) __builtin_amdgcn_raw_buffer_store_b128(sent, rs, WS_ML + idx * 128 + l * 16, 0, AUX_SC1);
    }
    lds_barrier();
    const unsigned long long tag = epoch + 1ull;
    const unsigned tlo = (unsigned)tag, thi = (unsigned)(tag >> 32);
    waitvm<0>();
    asm volatile("" ::"v"(touch));
    if (l == 0) __builtin_amdgcn_raw_buffer_store_b64(v2u{tlo, thi}, rs, WS_FLAG + idx * 128, 0, AUX_SC1);
    STAMP(2);
    lds_wait(L, S_QP, 8);
    STAMP(1);
    {
      const LAS f32x4* src = (const LAS f32x4*)(L + L_QRED) + l;
      f32x4 a = src[0];
#pragma unroll
      for (int n = 1; n < 16; ++n) a += src[n * 64];
      LAS float* t = (LAS float*)(L + L_QT) + (l >> 3) * 32 + (l & 7);
      t[0] = a.x; t[8] = a.y; t[16] = a.z; t[24] = a.w;
      asm volatile("s_waitcnt lgkmcnt(0)" ::: "memory");
      a = *((const LAS f32x4*)(L + L_QT) + l);
      __builtin_amdgcn_raw_buffer_store_b128(nosent(a), rs, qslot, 0, AUX_SC1);
    }
    {
      const int f1 = WS_FLAG + (kvh * 64 + l) * 128, f2 = WS_FLAG + ((br * 4 + cq) * 32 + (l & 31)) * 128;
      for (unsigned spin = 0;; ++spin) {
        asm volatile("" ::: "memory");
        const v2u x1 = __builtin_amdgcn_raw_buffer_load_b64(rs, f1, 0, AUX_SC1);
        const v2u x2 = __builtin_amdgcn_raw_buffer_load_b64(rs, f2, 0, AUX_SC1);
        if (__builtin_amdgcn_ballot_w64(((x1.x ^ tlo) | (x1.y ^ thi) | (x2.x ^ tlo) | (x2.y ^ thi)) != 0u) == 0ull || spin > SPIN_CAP) break;
        __builtin_amdgcn_s_sleep(8);
      }
      asm volatile("" ::: "memory");
    }
    STAMP(19);
    {
      const int qo = WS_QP + ((kvh * 4 + b) * 2 * 1024) * 4 + l * 16;
      v4u qg[16];
#pragma unroll
      for (int n = 0; n < 16; ++n) qg[n] = v4u{SENT, SENT, SENT, SENT};
      for (unsigned spin = 0;; ++spin) {
        asm volatile("" ::: "memory");
#pragma unroll
        for (int n = 0; n < 16; ++n)
          if (has_sent(qg[n])) qg[n] = __builtin_amdgcn_raw_buffer_load_b128(rs, qo + (n & 7) * 1024, (n >> 3) * (16 * 2 * 1024 * 4), AUX_SC1);
        bool bad = false;
#pragma unroll
        for (int n = 0; n < 16; ++n) bad |= has_sent(qg[n]);
        if (__builtin_amdgcn_ballot_w64(bad) == 0ull || spin > SPIN_CAP) break;
        __builtin_amdgcn_s_sleep(4);
      }
#pragma unroll
      for (int n = 0; n < 8; ++n) {
        const f32x4 q4 = __builtin_bit_cast(f32x4, qg[(n >> 2) * 8 + (n & 3)]) + __builtin_bit_cast(f32x4, qg[(n >> 2) * 8 + 4 + (n & 3)]);
        *(LAS v2u*)(L + L_QS + (2 * n + (l >> 5)) * 272 + (l & 31) * 8) = v2u{pk_bf16(q4.x, q4.y), pk_bf16(q4.z, q4.w)};
      }
    }
    lds_signal(L, S_Q, l);
    STAMP(3);
    if (idx == 0) {
      for (int pass = 0; pass < 4; ++pass)
        for (unsigned spin = 0;; ++spin) {
          asm volatile("" ::: "memory");
          const v2u x2 = __builtin_amdgcn_raw_buffer_load_b64(rs, WS_FLAG + (pass * 64 + l) * 128, 0, AUX_SC1);
          if (__builtin_amdgcn_ballot_w64(((x2.x ^ tlo) | (x2.y ^ thi)) != 0u) == 0ull || spin > SPIN_CAP) break;
          __builtin_amdgcn_s_sleep(8);
        }
      if (l == 0) __hip_atomic_store((GAS unsigned long long*)(ws + WS_EPOCH), tag, __ATOMIC_RELAXED, __HIP_MEMORY_SCOPE_AGENT);
    }
    lds_wait(L, S_LS, 8);
    if (l < 16) {
      const LAS f32x4* ls = (const LAS f32x4*)(L + L_LS + l * 32);
      const LAS f32x4* mx = (const LAS f32x4*)(L + L_MX + l * 32);
      const f32x4 l0 = ls[0], l1 = ls[1], m0 = mx[0], m1 = mx[1];
      const float lsum = ((l0.x + l0.y) + (l0.z + l0.w)) + ((l1.x + l1.y) + (l1.z + l1.w));
      const float mm = fmaxf(fmaxf(fmaxf(m0.x, m0.y), fmaxf(m0.z, m0.w)), fmaxf(fmaxf(m1.x, m1.y), fmaxf(m1.z, m1.w))) * C2;
      __builtin_amdgcn_raw_buffer_store_b64(v2u{min(__float_as_uint(mm), SENT - 1u), min(__float_as_uint(lsum), SENT - 1u)}, rs,
                                            WS_ML + idx * 128 + l * 8, 0, AUX_SC1);
    }
    STAMP(5);
    lds_barrier();
    {
      const int mo = WS_ML + (kvh * 64 + l) * 128 + (br * 8 + (h & 7)) * 8;
      v2u ml = {SENT, SENT};
      for (unsigned spin = 0;; ++spin) {
        asm volatile("" ::: "memory");
        if (max(ml.x, ml.y) == SENT) ml = __builtin_amdgcn_raw_buffer_load_b64(rs, mo, 0, AUX_SC1);
        if (__builtin_amdgcn_ballot_w64(max(ml.x, ml.y) == SENT) == 0ull || spin > SPIN_CAP) break;
        __builtin_amdgcn_s_sleep(4);
      }
      const float m = __uint_as_float(ml.x), ls = __uint_as_float(ml.y);
      float M = m;
      M = fmaxf(M, dpp<ROR8>(M)); M = fmaxf(M, dpp<ROR4>(M)); M = fmaxf(M, dpp<ROR2>(M)); M = fmaxf(M, dpp<ROR1>(M));
      const float e = __builtin_amdgcn_exp2f(m - M);
      float Ls = e * ls;
      Ls += dpp<ROR8>(Ls); Ls += dpp<ROR4>(Ls); Ls += dpp<ROR2>(Ls); Ls += dpp<ROR1>(Ls);
      *(LAS float*)(L + L_WGT + ((l >> 4) * 16 + (l & 3) * 4 + ((l >> 2) & 3)) * 4) = e / Ls;
    }
    lds_signal(L, S_W, l);
    STAMP(12);
    STAMP(22);
    STAMP_FLUSH();
    return;
  }

  const int qrow = 512 * (cb & 1) + 64 * w;
  const v4u xf = *(const GAS v4u*)((br ? xB : xA) + ((l & 3) * 1024 + qrow + 16 * g + 8 * ((l >> 3) & 1)) * 2);
  v4u wr[8];
  const __amdgpu_buffer_rsrc_t rwq = MKRS(br ? wqB : wqA, 8 << 20);
  const int wqp = (qrow + 8 * (l >> 3)) * 8192 + (kvh * 1024 + (cb >> 1) * 64) * 2 + (l & 7) * 16;
#pragma unroll
  for (int n = 0; n < WIN; ++n) wr[n] = LDB(rwq, wqp + n * 8192, W_AUX);
  SB();
  STAMP(17);
  asm volatile("s_barrier" ::: "memory");
  SB();
#pragma unroll
  for (int n = WIN; n < 8; ++n) { waitvm<WIN - 1>(); if (n == WIN) STAMP(16); wr[n] = LDB(rwq, wqp + n * 8192, W_AUX); SB(); }
  const int wbase = (b * 4 + kvh) * (8192 * 256) + (128 * s + 16 * w) * 256;
  const int koff = wbase + (4 * (rho & 3) + ((rho >> 2) & 1)) * 256 + g * 16 + 64 * (rho >> 3);
  const int voff = wbase + l * 16;
  constexpr int TS = 2048 * 256;
  v4u kf[20], vf[20], wo[8];
  const __amdgpu_buffer_rsrc_t rkA = MKRS(kA, 32 << 20), rkB = MKRS(kB, 32 << 20), rvA = MKRS(vA, 32 << 20), rvB = MKRS(vB, 32 << 20);
#define KLOAD(n) __builtin_amdgcn_raw_buffer_load_b128((n) < 16 ? rkA : rkB, koff + ((n) & 1) * 128 + (((n) >> 1) & 1) * 512, ((n) < 16 ? ((n) >> 2) : 3) * TS, KV_AUX)
#define VLOAD(n) __builtin_amdgcn_raw_buffer_load_b128((n) < 16 ? rvA : rvB, voff + ((n) & 3) * 1024, ((n) < 16 ? ((n) >> 2) : 3) * TS, KV_AUX)
#define PK(n) do { SB(); waitvm<WIN - 1>(); kf[n] = KLOAD(n); SB(); } while (0)
#define PV_(n) do { SB(); waitvm<WIN - 1>(); vf[n] = VLOAD(n); SB(); } while (0)
#define PVX(n) do { SB(); vf[n] = VLOAD(n); SB(); } while (0)
#pragma unroll
  for (int n = 0; n < WIN; ++n) PK(n);
  SB(); kf[WIN] = KLOAD(WIN); SB();

  {
    f32x4 qa[8];
#pragma unroll
    for (int dd = 0; dd < 8; ++dd) {
      const v4u a = {tr2(wr[0], wr[1], dd), tr2(wr[2], wr[3], dd), tr2(wr[4], wr[5], dd), tr2(wr[6], wr[7], dd)};
      qa[dd] = mfma32(a, xf, f32x4{0.f, 0.f, 0.f, 0.f});
    }
    if ((l & 12) == ((g >> 1) << 3)) {
      LAS f32x4* d = (LAS f32x4*)(L + L_QRED + ((w * 2 + (g >> 1)) * 4 + (l & 3)) * 256 + (g & 1) * 128);
#pragma unroll
      for (int dd = 0; dd < 8; ++dd) d[dd] = qa[dd];
    }
  }
  lds_signal(L, S_QP, l);
  STAMP(1);
#pragma unroll
  for (int n = WIN + 1; n < 20; ++n) PK(n);
#pragma unroll
  for (int n = 0; n < XPT; ++n) PV_(n);
  STAMP(4);
  lds_wait(L, S_Q, 1);
  STAMP(11);
  PVX(XPT);

  f32x4 sc[5];
  {
    const LAS char* qp = L + L_QS + (l & 7) * 272 + g * 16 + 64 * (rho >> 3);
    const v4u qa0 = *(const LAS v4u*)qp, qa1 = *(const LAS v4u*)(qp + 128);
    const v4u qb0 = *(const LAS v4u*)(qp + 8 * 272), qb1 = *(const LAS v4u*)(qp + 8 * 272 + 128);
#pragma unroll
    for (int u = 0; u < 5; ++u) {
      const f32x4 z = {0.f, 0.f, 0.f, 0.f};
      const f32x4 t1 = mfma32(kf[4 * u + 1], u < 4 ? qa1 : qb1, mfma32(kf[4 * u], u < 4 ? qa0 : qb0, z));
      const f32x4 t2 = mfma32(kf[4 * u + 3], u < 4 ? qa1 : qb1, mfma32(kf[4 * u + 2], u < 4 ? qa0 : qb0, z));
#pragma unroll
      for (int r = 0; r < 4; ++r) {
        auto sw = __builtin_amdgcn_permlane32_swap(__float_as_uint(t1[r]), __float_as_uint(t2[r]), false, false);
        sc[u][r] = __uint_as_float(sw[0]) + dpp<ROR8>(__uint_as_float(sw[1]));
      }
      if (u == 2) PVX(XPT + 1);
    }
  }
  {
    float mB = fmaxf(fmaxf(sc[4].x, sc[4].y), fmaxf(sc[4].z, sc[4].w));
    float mA = fmaxf(fmaxf(sc[0].x, sc[0].y), fmaxf(sc[0].z, sc[0].w));
#pragma unroll
    for (int u = 1; u < 4; ++u) mA = fmaxf(mA, fmaxf(fmaxf(sc[u].x, sc[u].y), fmaxf(sc[u].z, sc[u].w)));
    mA = xg_max(mA); mB = xg_max(mB);
    if (l < 8) {
      *(LAS float*)(L + L_MX + (l * 8 + w) * 4) = mA;
      *(LAS float*)(L + L_MX + 256 + (l * 8 + w) * 4) = mB;
    }
  }
  lds_signal(L, S_MX, l);
  STAMP(5);
  PV_(XPT + 2);
  PV_(XPT + 3);
  lds_wait(L, S_MX, 8);
  STAMP(12);
  PVX(XPT + 4);
  float MA, MB;
  {
    const LAS f32x4* m = (const LAS f32x4*)(L + L_MX + (l & 7) * 32);
    const f32x4 a0 = m[0], a1 = m[1], b0 = m[16], b1 = m[17];
    MA = fmaxf(fmaxf(fmaxf(a0.x, a0.y), fmaxf(a0.z, a0.w)), fmaxf(fmaxf(a1.x, a1.y), fmaxf(a1.z, a1.w))) * C2;
    MB = fmaxf(fmaxf(fmaxf(b0.x, b0.y), fmaxf(b0.z, b0.w)), fmaxf(fmaxf(b1.x, b1.y), fmaxf(b1.z, b1.w))) * C2;
  }
  unsigned pl[10], ph[10];
  {
    float la = 0.f, lb = 0.f;
#pragma unroll
    for (int u = 0; u < 5; ++u) {
      const float M = u < 4 ? MA : MB;
      const float p0 = __builtin_amdgcn_exp2f(fmaf(sc[u].x, C2, -M)), p1 = __builtin_amdgcn_exp2f(fmaf(sc[u].y, C2, -M));
      const float p2 = __builtin_amdgcn_exp2f(fmaf(sc[u].z, C2, -M)), p3 = __builtin_amdgcn_exp2f(fmaf(sc[u].w, C2, -M));
      const float t = (p0 + p1) + (p2 + p3);
      if (u < 4) la += t; else lb = t;
      pl[2 * u] = rho < 8 ? pk_bf16(p0, p1) : 0u; pl[2 * u + 1] = rho < 8 ? pk_bf16(p2, p3) : 0u;
      ph[2 * u] = __builtin_amdgcn_mov_dpp(pl[2 * u], ROR8, 0xf, 0xf, true);
      ph[2 * u + 1] = __builtin_amdgcn_mov_dpp(pl[2 * u + 1], ROR8, 0xf, 0xf, true);
      if (u == 2) PVX(XPT + 5);
    }
    la = x32_sum(x16_sum(la)); lb = x32_sum(x16_sum(lb));
    if (l < 8) {
      *(LAS float*)(L + L_LS + (l * 8 + w) * 4) = la;
      *(LAS float*)(L + L_LS + 256 + (l * 8 + w) * 4) = lb;
    }
  }
  lds_signal(L, S_LS, l);
  STAMP(13);
  PV_(XPT + 6);
  f32x4 acc[4];
#pragma unroll
  for (int q4 = 0; q4 < 4; ++q4) {
    const v4u a0 = {tr2(vf[0], vf[1], q4), tr2(vf[2], vf[3], q4), tr2(vf[4], vf[5], q4), tr2(vf[6], vf[7], q4)};
    const v4u a1 = {tr2(vf[0], vf[1], q4 + 4), tr2(vf[2], vf[3], q4 + 4), tr2(vf[4], vf[5], q4 + 4), tr2(vf[6], vf[7], q4 + 4)};
    acc[q4] = mfma32(a1, v4u{ph[0], ph[1], ph[2], ph[3]}, mfma32(a0, v4u{pl[0], pl[1], pl[2], pl[3]}, f32x4{0.f, 0.f, 0.f, 0.f}));
  }
#pragma unroll
  for (int n = XPT + 7; n < 20; ++n) PV_(n);
  const __amdgpu_buffer_rsrc_t rwo = MKRS(br ? woB : woA, 8 << 20);
  const int wop = (128 * h + 32 * (w >> 1) + 4 * (w & 1) + g) * 2048 + 512 * cq + rho * 16;
#define WLOAD(n) LDB(rwo, wop + ((n) >> 1) * (8 * 2048) + ((n) & 1) * 256, W_AUX)
#define PW(n) do { SB(); waitvm<WIN - 1>(); wo[n] = WLOAD(n); SB(); } while (0)
  SB(); wo[0] = WLOAD(0); SB();
  STAMP(14);
#pragma unroll
  for (int q4 = 0; q4 < 4; ++q4) {
    const v4u a0 = {tr2(vf[8], vf[9], q4), tr2(vf[10], vf[11], q4), tr2(vf[12], vf[13], q4), tr2(vf[14], vf[15], q4)};
    const v4u a1 = {tr2(vf[8], vf[9], q4 + 4), tr2(vf[10], vf[11], q4 + 4), tr2(vf[12], vf[13], q4 + 4), tr2(vf[14], vf[15], q4 + 4)};
    acc[q4] = mfma32(a1, v4u{ph[4], ph[5], ph[6], ph[7]}, mfma32(a0, v4u{pl[4], pl[5], pl[6], pl[7]}, acc[q4]));
  }
  LAS f32x4* dA = (LAS f32x4*)(L + L_RED + (w * 8 + (l & 7)) * 528 + g * 128 + (rho >> 3) * 64);
#pragma unroll
  for (int q4 = 0; q4 < 4; ++q4) dA[q4] = acc[q4];
  PW(1); PW(2); PW(3);
  STAMP(20);
#pragma unroll
  for (int q4 = 0; q4 < 4; ++q4) {
    const v2u a0 = {tr2(vf[16], vf[17], q4), tr2(vf[18], vf[19], q4)};
    const v2u a1 = {tr2(vf[16], vf[17], q4 + 4), tr2(vf[18], vf[19], q4 + 4)};
    dA[8 * 8 * 33 + q4] = mfma16(a1, v2u{ph[8], ph[9]}, mfma16(a0, v2u{pl[8], pl[9]}, f32x4{0.f, 0.f, 0.f, 0.f}));
  }
  SB(); wo[4] = WLOAD(4); SB();
  STAMP(6);
  lds_barrier();
  STAMP(15);
  {
    const int pb = tid >> 8, hd = (tid >> 5) & 7, quad = tid & 31;
    const LAS char* src = L + L_RED + pb * (8 * 8 * 528) + hd * 528 + quad * 16;
    f32x4 a = *(const LAS f32x4*)src;
#pragma unroll
    for (int ww = 1; ww < 8; ++ww) a += *(const LAS f32x4*)(src + ww * (8 * 528));
    const int rec = (pb * 32 + kvh * 8 + hd) * 64 + b * 16 + s;
    __builtin_amdgcn_raw_buffer_store_b128(nosent(a), rs, WS_PO + rec * 512 + quad * 16, 0, AUX_SC1);
  }
  STAMP(7);
  SB();
#pragma unroll
  for (int n = 5; n < 8; ++n) wo[n] = WLOAD(n);
  SB();
  v2u wt[16];
#pragma unroll
  for (int m = 0; m < 16; ++m) {
    wt[m] = v2u{tr2(wo[m >> 3], wo[2 + (m >> 3)], m & 7), tr2(wo[4 + (m >> 3)], wo[6 + (m >> 3)], m & 7)};
    asm volatile("" : "+v"(wt[m]));
  }
  const int sg = (l >> 2) & 3;
  STAMP(21);
  SB();

  {
    const int po = WS_PO + ((br * 32 + h) * 64 + (l & 3) * 16 + sg) * 512 + (16 * w + 4 * g) * 4;
    v4u pv[4];
#pragma unroll
    for (int n = 0; n < 4; ++n) pv[n] = __builtin_amdgcn_raw_buffer_load_b128(rs, po + n * 2048, 0, AUX_SC1);
    SB();
    lds_wait(L, S_W, 1);
    const f32x4 wg = *(const LAS f32x4*)(L + L_WGT + ((l & 3) * 16 + sg * 4) * 4);
    SB();
    for (unsigned spin = 0;; ++spin) {
      const bool bad = has_sent(pv[0]) | has_sent(pv[1]) | has_sent(pv[2]) | has_sent(pv[3]);
      if (__builtin_amdgcn_ballot_w64(bad) == 0ull || spin > SPIN_CAP) break;
      asm volatile("" ::: "memory");
      if (bad) {
#pragma unroll
        for (int n = 0; n < 4; ++n) pv[n] = __builtin_amdgcn_raw_buffer_load_b128(rs, po + n * 2048, 0, AUX_SC1);
      }
    }
    STAMP(8);
    f32x4 o = __builtin_bit_cast(f32x4, pv[0]) * wg.x + __builtin_bit_cast(f32x4, pv[1]) * wg.y +
              (__builtin_bit_cast(f32x4, pv[2]) * wg.z + __builtin_bit_cast(f32x4, pv[3]) * wg.w);
#pragma unroll
    for (int k = 0; k < 4; ++k) { float t = o[k]; t += dpp<ROR8>(t); t += dpp<ROR4>(t); o[k] = t; }
    const unsigned o01 = pk_bf16(o.x, o.y), o23 = pk_bf16(o.z, o.w);
    v2u bm[4];
#pragma unroll
    for (int k = 0; k < 4; ++k) bm[k] = v2u{sg == k ? o01 : 0u, sg == k ? o23 : 0u};
    LAS f32x4* d = (LAS f32x4*)(L + L_RED2 + (w * 4 + (l & 3)) * 1056 + g * 128 + sg * 16);
#pragma unroll
    for (int a = 0; a < 4; ++a) {
      f32x4 z = {0.f, 0.f, 0.f, 0.f};
#pragma unroll
      for (int k = 0; k < 4; ++k) z = mfma16(wt[4 * a + k], bm[k], z);
      d[(a >> 1) * 32 + (a & 1) * 4] = z;
    }
  }
  STAMP(18);
  lds_barrier();
  if (tid < 256) {
    const int bb = tid >> 6, quad = tid & 63;
    const LAS char* src = L + L_RED2 + bb * 1056 + quad * 16;
    f32x4 a = *(const LAS f32x4*)src;
#pragma unroll
    for (int ww = 1; ww < 8; ++ww) a += *(const LAS f32x4*)(src + ww * (4 * 1056));
    const int cons = (br * 4 + cq) * 32 + bb * 8 + (quad >> 3);
    __builtin_amdgcn_raw_buffer_store_b128(nosent(a), rs, WS_OUT + cons * 4096 + h * 128 + (quad & 7) * 16, 0, AUX_SC1);
  }
  STAMP(9);
  if (w == 7) {
    const int io = WS_OUT + idx * 4096 + l * 16;
    v4u pv[4];
    for (unsigned spin = 0;; ++spin) {
      asm volatile("" ::: "memory");
#pragma unroll
      for (int n = 0; n < 4; ++n) pv[n] = __builtin_amdgcn_raw_buffer_load_b128(rs, io + n * 1024, 0, AUX_SC1);
      const bool bad = has_sent(pv[0]) | has_sent(pv[1]) | has_sent(pv[2]) | has_sent(pv[3]);
      if (__builtin_amdgcn_ballot_w64(bad) == 0ull || spin > SPIN_CAP) break;
      __builtin_amdgcn_s_sleep(1);
    }
    f32x4 a = (__builtin_bit_cast(f32x4, pv[0]) + __builtin_bit_cast(f32x4, pv[1])) +
              (__builtin_bit_cast(f32x4, pv[2]) + __builtin_bit_cast(f32x4, pv[3]));
#pragma unroll
    for (int k = 0; k < 4; ++k) { float t = a[k]; t += dpp<ROR8>(t); a[k] = x32_sum(x16_sum(t)); }
    const int r = (l >> 3) & 3;
    const float v = r == 0 ? a.x : r == 1 ? a.y : r == 2 ? a.z : a.w;
    const int obr = idx >> 7, ocq = (idx >> 5) & 3, ob = (idx >> 3) & 3, oc = idx & 7;
    if (l < 32) out[ob * 2048 + obr * 1024 + ocq * 256 + oc * 32 + l] = (unsigned short)(pk_bf16(v, v) & 0xFFFFu);
    STAMP(10);
  }
  STAMP(22);
  STAMP_FLUSH();
}

extern "C" void kernel_launch(void* const* d_in, const int* in_sizes, int n_in, void* d_out, int out_size, void* d_ws,
                              size_t ws_size, hipStream_t stream) {
  hipLaunchKernelGGL(fused_decode, dim3(256), dim3(576), 0, stream, (const char*)d_in[0], (const char*)d_in[1],
                     (const char*)d_in[2], (const char*)d_in[3], (const char*)d_in[4], (const char*)d_in[5],
                     (const char*)d_in[6], (const char*)d_in[7], (const char*)d_in[8], (const char*)d_in[9],
                     (unsigned short*)d_out, (char*)d_ws);
}
```

```cpp
#define DIAG 0
#include <hip/hip_runtime.h>
#include <stdint.h>

#define KV_AUX 2
#define W_AUX 2
#define WIN 4
#define XPT 8
#define PRETOUCH 1
#define STAMPS 0

typedef unsigned v4u __attribute__((ext_vector_type(4)));
typedef unsigned v2u __attribute__((ext_vector_type(2)));
typedef float f32x4 __attribute__((ext_vector_type(4)));
typedef __bf16 bf16x8 __attribute__((ext_vector_type(8)));
typedef short s16x4 __attribute__((ext_vector_type(4)));
#define GAS __attribute__((address_space(1)))
#define LAS __attribute__((address_space(3)))
#define SB() __builtin_amdgcn_sched_barrier(0)

constexpr int WS_EPOCH = 0;
constexpr int WS_FLAG = 4096;
constexpr int WS_QP = WS_FLAG + 256 * 128;
constexpr int WS_ML = WS_QP + 2 * 4 * 4 * 2 * 1024 * 4;
constexpr int WS_PO = WS_ML + 256 * 128;
constexpr int WS_OUT = WS_PO + 2 * 32 * 64 * 128 * 4;
constexpr int WS_END = WS_OUT + 256 * 32 * 32 * 4;
constexpr int WS_STAMP = WS_END;
constexpr int AUX_SC1 = 16;
constexpr unsigned SENT = 0xFFFFFFFFu;
constexpr unsigned SPIN_CAP = 1u << 16;

constexpr int L_SYNC = 0;
constexpr int L_WGT = 64;
constexpr int L_QRED = 512;
constexpr int L_QT = L_QRED + 16384;
constexpr int L_QS = L_QT + 1024;
constexpr int L_MX = L_QS + 2 * 8 * 272;
constexpr int L_LS = L_MX + 512;
constexpr int L_RED = L_LS + 512;
constexpr int L_RED2 = L_RED + 2 * 8 * 8 * 528;
constexpr int L_END = L_RED2 + 8 * 4 * 1056;
constexpr int S_QP = 0, S_Q = 4, S_MX = 8, S_LS = 12, S_W = 16;

#define MKRS(ptr, n) __builtin_amdgcn_make_buffer_rsrc((void*)(ptr), 0, (n), 0x00020000)
#define LDB(rs, off, aux) __builtin_amdgcn_raw_buffer_load_b128((rs), (off), 0, (aux))
__device__ __forceinline__ f32x4 mfma32(v4u a, v4u b, f32x4 c) {
  return __builtin_amdgcn_mfma_f32_16x16x32_bf16(__builtin_bit_cast(bf16x8, a), __builtin_bit_cast(bf16x8, b), c, 0, 0, 0);
}
__device__ __forceinline__ f32x4 mfma16(v2u a, v2u b, f32x4 c) {
  return __builtin_amdgcn_mfma_f32_16x16x16bf16_1k(__builtin_bit_cast(s16x4, a), __builtin_bit_cast(s16x4, b), c, 0, 0, 0);
}
__device__ __forceinline__ unsigned pk_bf16(float lo, float hi) {
  typedef __bf16 bf16x2 __attribute__((ext_vector_type(2)));
  typedef float f32x2 __attribute__((ext_vector_type(2)));
  f32x2 v = {lo, hi};
  return __builtin_bit_cast(unsigned, __builtin_convertvector(v, bf16x2));
}
__device__ __forceinline__ unsigned tr2(const v4u& r0, const v4u& r1, int dd) {
  return __builtin_amdgcn_perm(r1[dd >> 1], r0[dd >> 1], (dd & 1) ? 0x07060302u : 0x05040100u);
}
__device__ __forceinline__ float xg_max(float x) {
  auto s = __builtin_amdgcn_permlane16_swap(__float_as_uint(x), __float_as_uint(x), false, false);
  x = fmaxf(__uint_as_float(s[0]), __uint_as_float(s[1]));
  auto t = __builtin_amdgcn_permlane32_swap(__float_as_uint(x), __float_as_uint(x), false, false);
  return fmaxf(__uint_as_float(t[0]), __uint_as_float(t[1]));
}
__device__ __forceinline__ float x16_sum(float x) {
  auto s = __builtin_amdgcn_permlane16_swap(__float_as_uint(x), __float_as_uint(x), false, false);
  return __uint_as_float(s[0]) + __uint_as_float(s[1]);
}
__device__ __forceinline__ float x32_sum(float x) {
  auto t = __builtin_amdgcn_permlane32_swap(__float_as_uint(x), __float_as_uint(x), false, false);
  return __uint_as_float(t[0]) + __uint_as_float(t[1]);
}
template <int CTRL> __device__ __forceinline__ float dpp(float x) {
  return __builtin_bit_cast(float, __builtin_amdgcn_mov_dpp(__builtin_bit_cast(int, x), CTRL, 0xf, 0xf, true));
}
constexpr int ROR1 = 0x121, ROR2 = 0x122, ROR4 = 0x124, ROR8 = 0x128;
__device__ __forceinline__ v4u nosent(f32x4 v) {
  v4u u = __builtin_bit_cast(v4u, v);
  u.x = min(u.x, SENT - 1u); u.y = min(u.y, SENT - 1u); u.z = min(u.z, SENT - 1u); u.w = min(u.w, SENT - 1u);
  return u;
}
__device__ __forceinline__ bool has_sent(v4u u) { return max(max(u.x, u.y), max(u.z, u.w)) == SENT; }
__device__ __forceinline__ void lds_barrier() {
  asm volatile("s_waitcnt lgkmcnt(0)\n\ts_barrier" ::: "memory");
}
template <int N> __device__ __forceinline__ void waitvm() { asm volatile("s_waitcnt vmcnt(%0)" ::"i"(N) : "memory"); }
__device__ __forceinline__ void lds_signal(LAS char* L, int word, int l) {
  asm volatile("s_waitcnt lgkmcnt(0)" ::: "memory");
  if (l == 0) __hip_atomic_fetch_add((LAS unsigned*)(L + L_SYNC + word), 1u, __ATOMIC_RELAXED, __HIP_MEMORY_SCOPE_WORKGROUP);
  asm volatile("" ::: "memory");
}
__device__ __forceinline__ void lds_wait(LAS char* L, int word, unsigned want) {
  for (;;) {
    const unsigned v = __hip_atomic_load((LAS unsigned*)(L + L_SYNC + word), __ATOMIC_RELAXED, __HIP_MEMORY_SCOPE_WORKGROUP);
    if (__builtin_amdgcn_readfirstlane(v) >= want) break;
    __builtin_amdgcn_s_sleep(1);
  }
  asm volatile("" ::: "memory");
}

#define STAMP(n) do { if ((n) == 0 || (n) == DIAG) { SB(); ts[(n) == 0 ? 0 : 1] = __builtin_amdgcn_s_memrealtime(); SB(); } } while (0)
#define STAMP_FLUSH() do { if (w == 7) { const unsigned long long tgt = (DIAG ? ts[1] : ts[0]) + 4000ull; while (__builtin_amdgcn_s_memrealtime() < tgt) __builtin_amdgcn_s_sleep(2); } } while (0)

__global__ void __launch_bounds__(576, 3)
fused_decode(const char* a0, const char* a1, const char* a2, const char* a3, const char* a4, const char* a5, const char* a6, const char* a7,
             const char* a8, const char* a9, unsigned short* a10, char* a11) {
  unsigned long long tfirst;
  asm volatile("s_memrealtime %0\n\ts_waitcnt lgkmcnt(0)" : "=s"(tfirst) :: "memory");
  typedef char* __attribute__((address_space(4))) const* kap_t;
  kap_t ka = (kap_t)__builtin_amdgcn_kernarg_segment_ptr();
  asm volatile("" : "+s"(ka));
  const char *xA = ka[0], *kA = ka[1], *vA = ka[2], *wqA = ka[3], *woA = ka[4], *xB = ka[5], *kB = ka[6], *vB = ka[7], *wqB = ka[8], *woB = ka[9];
  unsigned short* out = (unsigned short*)ka[10]; char* ws = ka[11];
  __shared__ __attribute__((aligned(16))) char smem[L_END];
  LAS char* L = (LAS char*)smem;
  const int c = blockIdx.x, tid = threadIdx.x, l = tid & 63, w = __builtin_amdgcn_readfirstlane(tid >> 6);
  const int idx = (c & 7) * 32 + (c >> 3), kvh = idx >> 6, gi = idx & 63;
  const int br = gi >> 5, cb = gi & 31;
  const int b = gi >> 4, s = gi & 15;
  const int h = kvh * 8 + ((gi >> 2) & 7), cq = gi & 3;
  const int g = l >> 4, rho = l & 15;
  const __amdgpu_buffer_rsrc_t rs = __builtin_amdgcn_make_buffer_rsrc((void*)ws, 0, WS_END, 0x00020000);
  unsigned long long ts[2] = {0, 0};
  ts[0] = tfirst;

  constexpr float C2 = 0.08838834764831845f * 1.4426950408889634f;

  if (w == 8) {
    __builtin_amdgcn_s_setprio(3);
    const unsigned long long epoch =
        __hip_atomic_load((GAS unsigned long long*)(ws + WS_EPOCH), __ATOMIC_RELAXED, __HIP_MEMORY_SCOPE_AGENT);
    if (l < 16) *(LAS unsigned*)(L + L_SYNC + l * 4) = 0u;
    unsigned touch = 0;
    {
      const size_t cub = (size_t)(b * 4 + kvh) * (8192 * 256) + (size_t)(128 * s) * 256;
      const int t = l & 15;
      const char* p = t < 4 ? kA + cub + t * (2048 * 256) : t < 8 ? vA + cub + (t - 4) * (2048 * 256)
                    : t == 8 ? kB + cub + 6144 * 256 : t == 9 ? vB + cub + 6144 * 256
                    : t < 14 ? (br ? woB : woA) + (size_t)(128 * h + 32 * (t - 10)) * 2048 + 512 * cq
                    : t == 14 ? (const char*)out : ws + WS_OUT + ((br * 4 + cq) * 32) * 4096;
      if (l < 16) touch = __builtin_nontemporal_load((const GAS unsigned*)p);
    }
    const int qslot = WS_QP + ((((br * 4 + kvh) * 4 + (l >> 4)) * 2 + (cb & 1)) * 1024 + (cb >> 1) * 64 + (l & 15) * 4) * 4;
    {
      const v4u sent = {SENT, SENT, SENT, SENT};
      __builtin_amdgcn_raw_buffer_store_b128(sent, rs, qslot, 0, AUX_SC1);
      const int po = WS_PO + ((br * 32 + h) * 64 + cq * 16) * 512 + l * 16;
#pragma unroll
      for (int n = 0; n < 8; ++n) __builtin_amdgcn_raw_buffer_store_b128(sent, rs, po + n * 1024, 0, AUX_SC1);
      const int oo = WS_OUT + idx * 4096 + l * 16;
#pragma unroll
      for (int n = 0; n < 4; ++n) __builtin_amdgcn_raw_buffer_store_b128(sent, rs, oo + n * 1024, 0, AUX_SC1);
      if (l < 8) __builtin_amdgcn_raw_buffer_store_b128(sent, rs, WS_ML + idx * 128 + l * 16, 0, AUX_SC1);
    }
    lds_barrier();
    const unsigned long long tag = epoch + 1ull;
    const unsigned tlo = (unsigned)tag, thi = (unsigned)(tag >> 32);
    waitvm<0>();
    asm volatile("" ::"v"(touch));
    if (l == 0) __builtin_amdgcn_raw_buffer_store_b64(v2u{tlo, thi}, rs, WS_FLAG + idx * 128, 0, AUX_SC1);
    STAMP(2);
    lds_wait(L, S_QP, 8);
    STAMP(1);
    {
      const LAS f32x4* src = (const LAS f32x4*)(L + L_QRED) + l;
      f32x4 a = src[0];
#pragma unroll
      for (int n = 1; n < 16; ++n) a += src[n * 64];
      LAS float* t = (LAS float*)(L + L_QT) + (l >> 3) * 32 + (l & 7);
      t[0] = a.x; t[8] = a.y; t[16] = a.z; t[24] = a.w;
      asm volatile("s_waitcnt lgkmcnt(0)" ::: "memory");
      a = *((const LAS f32x4*)(L + L_QT) + l);
      __builtin_amdgcn_raw_buffer_store_b128(nosent(a), rs, qslot, 0, AUX_SC1);
    }
    {
      const int f1 = WS_FLAG + (kvh * 64 + l) * 128, f2 = WS_FLAG + ((br * 4 + cq) * 32 + (l & 31)) * 128;
      for (unsigned spin = 0;; ++spin) {
        asm volatile("" ::: "memory");
        const v2u x1 = __builtin_amdgcn_raw_buffer_load_b64(rs, f1, 0, AUX_SC1);
        const v2u x2 = __builtin_amdgcn_raw_buffer_load_b64(rs, f2, 0, AUX_SC1);
        if (__builtin_amdgcn_ballot_w64(((x1.x ^ tlo) | (x1.y ^ thi) | (x2.x ^ tlo) | (x2.y ^ thi)) != 0u) == 0ull || spin > SPIN_CAP) break;
        __builtin_amdgcn_s_sleep(8);
      }
      asm volatile("" ::: "memory");
    }
    STAMP(19);
    {
      const int qo = WS_QP + ((kvh * 4 + b) * 2 * 1024) * 4 + l * 16;
      v4u qg[16];
#pragma unroll
      for (int n = 0; n < 16; ++n) qg[n] = v4u{SENT, SENT, SENT, SENT};
      for (unsigned spin = 0;; ++spin) {
        asm volatile("" ::: "memory");
#pragma unroll
        for (int n = 0; n < 16; ++n)
          if (has_sent(qg[n])) qg[n] = __builtin_amdgcn_raw_buffer_load_b128(rs, qo + (n & 7) * 1024, (n >> 3) * (16 * 2 * 1024 * 4), AUX_SC1);
        bool bad = false;
#pragma unroll
        for (int n = 0; n < 16; ++n) bad |= has_sent(qg[n]);
        if (__builtin_amdgcn_ballot_w64(bad) == 0ull || spin > SPIN_CAP) break;
        __builtin_amdgcn_s_sleep(4);
      }
#pragma unroll
      for (int n = 0; n < 8; ++n) {
        const f32x4 q4 = __builtin_bit_cast(f32x4, qg[(n >> 2) * 8 + (n & 3)]) + __builtin_bit_cast(f32x4, qg[(n >> 2) * 8 + 4 + (n & 3)]);
        *(LAS v2u*)(L + L_QS + (2 * n + (l >> 5)) * 272 + (l & 31) * 8) = v2u{pk_bf16(q4.x, q4.y), pk_bf16(q4.z, q4.w)};
      }
    }
    lds_signal(L, S_Q, l);
    STAMP(3);
    if (idx == 0) {
      for (int pass = 0; pass < 4; ++pass)
        for (unsigned spin = 0;; ++spin) {
          asm volatile("" ::: "memory");
          const v2u x2 = __builtin_amdgcn_raw_buffer_load_b64(rs, WS_FLAG + (pass * 64 + l) * 128, 0, AUX_SC1);
          if (__builtin_amdgcn_ballot_w64(((x2.x ^ tlo) | (x2.y ^ thi)) != 0u) == 0ull || spin > SPIN_CAP) break;
          __builtin_amdgcn_s_sleep(8);
        }
      if (l == 0) __hip_atomic_store((GAS unsigned long long*)(ws + WS_EPOCH), tag, __ATOMIC_RELAXED, __HIP_MEMORY_SCOPE_AGENT);
    }
    lds_wait(L, S_LS, 8);
    if (l < 16) {
      const LAS f32x4* ls = (const LAS f32x4*)(L + L_LS + l * 32);
      const LAS f32x4* mx = (const LAS f32x4*)(L + L_MX + l * 32);
      const f32x4 l0 = ls[0], l1 = ls[1], m0 = mx[0], m1 = mx[1];
      const float lsum = ((l0.x + l0.y) + (l0.z + l0.w)) + ((l1.x + l1.y) + (l1.z + l1.w));
      const float mm = fmaxf(fmaxf(fmaxf(m0.x, m0.y), fmaxf(m0.z, m0.w)), fmaxf(fmaxf(m1.x, m1.y), fmaxf(m1.z, m1.w))) * C2;
      __builtin_amdgcn_raw_buffer_store_b64(v2u{min(__float_as_uint(mm), SENT - 1u), min(__float_as_uint(lsum), SENT - 1u)}, rs,
                                            WS_ML + idx * 128 + l * 8, 0, AUX_SC1);
    }
    STAMP(5);
    lds_barrier();
    {
      const int mo = WS_ML + (kvh * 64 + l) * 128 + (br * 8 + (h & 7)) * 8;
      v2u ml = {SENT, SENT};
      for (unsigned spin = 0;; ++spin) {
        asm volatile("" ::: "memory");
        if (max(ml.x, ml.y) == SENT) ml = __builtin_amdgcn_raw_buffer_load_b64(rs, mo, 0, AUX_SC1);
        if (__builtin_amdgcn_ballot_w64(max(ml.x, ml.y) == SENT) == 0ull || spin > SPIN_CAP) break;
        __builtin_amdgcn_s_sleep(4);
      }
      const float m = __uint_as_float(ml.x), ls = __uint_as_float(ml.y);
      float M = m;
      M = fmaxf(M, dpp<ROR8>(M)); M = fmaxf(M, dpp<ROR4>(M)); M = fmaxf(M, dpp<ROR2>(M)); M = fmaxf(M, dpp<ROR1>(M));
      const float e = __builtin_amdgcn_exp2f(m - M);
      float Ls = e * ls;
      Ls += dpp<ROR8>(Ls); Ls += dpp<ROR4>(Ls); Ls += dpp<ROR2>(Ls); Ls += dpp<ROR1>(Ls);
      *(LAS float*)(L + L_WGT + ((l >> 4) * 16 + (l & 3) * 4 + ((l >> 2) & 3)) * 4) = e / Ls;
    }
    lds_signal(L, S_W, l);
    STAMP(12);
    STAMP(22);
    STAMP_FLUSH();
    return;
  }

  const int qrow = 512 * (cb & 1) + 64 * w;
  const v4u xf = *(const GAS v4u*)((br ? xB : xA) + ((l & 3) * 1024 + qrow + 16 * g + 8 * ((l >> 3) & 1)) * 2);
  v4u wr[8];
  const __amdgpu_buffer_rsrc_t rwq = MKRS(br ? wqB : wqA, 8 << 20);
  const int wqp = (qrow + 8 * (l >> 3)) * 8192 + (kvh * 1024 + (cb >> 1) * 64) * 2 + (l & 7) * 16;
#pragma unroll
  for (int n = 0; n < WIN; ++n) wr[n] = LDB(rwq, wqp + n * 8192, W_AUX);
  SB();
  STAMP(17);
  asm volatile("s_barrier" ::: "memory");
  SB();
#pragma unroll
  for (int n = WIN; n < 8; ++n) { waitvm<WIN - 1>(); if (n == WIN) STAMP(16); wr[n] = LDB(rwq, wqp + n * 8192, W_AUX); SB(); }
  const int wbase = (b * 4 + kvh) * (8192 * 256) + (128 * s + 16 * w) * 256;
  const int koff = wbase + (4 * (rho & 3) + ((rho >> 2) & 1)) * 256 + g * 16 + 64 * (rho >> 3);
  const int voff = wbase + l * 16;
  constexpr int TS = 2048 * 256;
  v4u kf[20], vf[20], wo[8];
  const __amdgpu_buffer_rsrc_t rkA = MKRS(kA, 32 << 20), rkB = MKRS(kB, 32 << 20), rvA = MKRS(vA, 32 << 20), rvB = MKRS(vB, 32 << 20);
#define KLOAD(n) __builtin_amdgcn_raw_buffer_load_b128((n) < 16 ? rkA : rkB, koff + ((n) & 1) * 128 + (((n) >> 1) & 1) * 512, ((n) < 16 ? ((n) >> 2) : 3) * TS, KV_AUX)
#define VLOAD(n) __builtin_amdgcn_raw_buffer_load_b128((n) < 16 ? rvA : rvB, voff + ((n) & 3) * 1024, ((n) < 16 ? ((n) >> 2) : 3) * TS, KV_AUX)
#define PK(n) do { SB(); waitvm<WIN - 1>(); kf[n] = KLOAD(n); SB(); } while (0)
#define PV_(n) do { SB(); waitvm<WIN - 1>(); vf[n] = VLOAD(n); SB(); } while (0)
#define PVX(n) do { SB(); vf[n] = VLOAD(n); SB(); } while (0)
#pragma unroll
  for (int n = 0; n < WIN; ++n) PK(n);
  SB(); kf[WIN] = KLOAD(WIN); SB();

  {
    f32x4 qa[8];
#pragma unroll
    for (int dd = 0; dd < 8; ++dd) {
      const v4u a = {tr2(wr[0], wr[1], dd), tr2(wr[2], wr[3], dd), tr2(wr[4], wr[5], dd), tr2(wr[6], wr[7], dd)};
      qa[dd] = mfma32(a, xf, f32x4{0.f, 0.f, 0.f, 0.f});
    }
    if ((l & 12) == ((g >> 1) << 3)) {
      LAS f32x4* d = (LAS f32x4*)(L + L_QRED + ((w * 2 + (g >> 1)) * 4 + (l & 3)) * 256 + (g & 1) * 128);
#pragma unroll
      for (int dd = 0; dd < 8; ++dd) d[dd] = qa[dd];
    }
  }
  lds_signal(L, S_QP, l);
  STAMP(1);
#pragma unroll
  for (int n = WIN + 1; n < 20; ++n) PK(n);
#pragma unroll
  for (int n = 0; n < XPT; ++n) PV_(n);
  STAMP(4);
  lds_wait(L, S_Q, 1);
  STAMP(11);
  PVX(XPT);

  f32x4 sc[5];
  {
    const LAS char* qp = L + L_QS + (l & 7) * 272 + g * 16 + 64 * (rho >> 3);
    const v4u qa0 = *(const LAS v4u*)qp, qa1 = *(const LAS v4u*)(qp + 128);
    const v4u qb0 = *(const LAS v4u*)(qp + 8 * 272), qb1 = *(const LAS v4u*)(qp + 8 * 272 + 128);
#pragma unroll
    for (int u = 0; u < 5; ++u) {
      const f32x4 z = {0.f, 0.f, 0.f, 0.f};
      const f32x4 t1 = mfma32(kf[4 * u + 1], u < 4 ? qa1 : qb1, mfma32(kf[4 * u], u < 4 ? qa0 : qb0, z));
      const f32x4 t2 = mfma32(kf[4 * u + 3], u < 4 ? qa1 : qb1, mfma32(kf[4 * u + 2], u < 4 ? qa0 : qb0, z));
#pragma unroll
      for (int r = 0; r < 4; ++r) {
        auto sw = __builtin_amdgcn_permlane32_swap(__float_as_uint(t1[r]), __float_as_uint(t2[r]), false, false);
        sc[u][r] = __uint_as_float(sw[0]) + dpp<ROR8>(__uint_as_float(sw[1]));
      }
      if (u == 2) PVX(XPT + 1);
    }
  }
  {
    float mB = fmaxf(fmaxf(sc[4].x, sc[4].y), fmaxf(sc[4].z, sc[4].w));
    float mA = fmaxf(fmaxf(sc[0].x, sc[0].y), fmaxf(sc[0].z, sc[0].w));
#pragma unroll
    for (int u = 1; u < 4; ++u) mA = fmaxf(mA, fmaxf(fmaxf(sc[u].x, sc[u].y), fmaxf(sc[u].z, sc[u].w)));
    mA = xg_max(mA); mB = xg_max(mB);
    if (l < 8) {
      *(LAS float*)(L + L_MX + (l * 8 + w) * 4) = mA;
      *(LAS float*)(L + L_MX + 256 + (l * 8 + w) * 4) = mB;
    }
  }
  lds_signal(L, S_MX, l);
  STAMP(5);
  PV_(XPT + 2);
  PV_(XPT + 3);
  lds_wait(L, S_MX, 8);
  STAMP(12);
  PVX(XPT + 4);
  float MA, MB;
  {
    const LAS f32x4* m = (const LAS f32x4*)(L + L_MX + (l & 7) * 32);
    const f32x4 a0 = m[0], a1 = m[1], b0 = m[16], b1 = m[17];
    MA = fmaxf(fmaxf(fmaxf(a0.x, a0.y), fmaxf(a0.z, a0.w)), fmaxf(fmaxf(a1.x, a1.y), fmaxf(a1.z, a1.w))) * C2;
    MB = fmaxf(fmaxf(fmaxf(b0.x, b0.y), fmaxf(b0.z, b0.w)), fmaxf(fmaxf(b1.x, b1.y), fmaxf(b1.z, b1.w))) * C2;
  }
  unsigned pl[10], ph[10];
  {
    float la = 0.f, lb = 0.f;
#pragma unroll
    for (int u = 0; u < 5; ++u) {
      const float M = u < 4 ? MA : MB;
      const float p0 = __builtin_amdgcn_exp2f(fmaf(sc[u].x, C2, -M)), p1 = __builtin_amdgcn_exp2f(fmaf(sc[u].y, C2, -M));
      const float p2 = __builtin_amdgcn_exp2f(fmaf(sc[u].z, C2, -M)), p3 = __builtin_amdgcn_exp2f(fmaf(sc[u].w, C2, -M));
      const float t = (p0 + p1) + (p2 + p3);
      if (u < 4) la += t; else lb = t;
      pl[2 * u] = rho < 8 ? pk_bf16(p0, p1) : 0u; pl[2 * u + 1] = rho < 8 ? pk_bf16(p2, p3) : 0u;
      ph[2 * u] = __builtin_amdgcn_mov_dpp(pl[2 * u], ROR8, 0xf, 0xf, true);
      ph[2 * u + 1] = __builtin_amdgcn_mov_dpp(pl[2 * u + 1], ROR8, 0xf, 0xf, true);
      if (u == 2) PVX(XPT + 5);
    }
    la = x32_sum(x16_sum(la)); lb = x32_sum(x16_sum(lb));
    if (l < 8) {
      *(LAS float*)(L + L_LS + (l * 8 + w) * 4) = la;
      *(LAS float*)(L + L_LS + 256 + (l * 8 + w) * 4) = lb;
    }
  }
  lds_signal(L, S_LS, l);
  STAMP(13);
  PV_(XPT + 6);
  f32x4 acc[4];
#pragma unroll
  for (int q4 = 0; q4 < 4; ++q4) {
    const v4u a0 = {tr2(vf[0], vf[1], q4), tr2(vf[2], vf[3], q4), tr2(vf[4], vf[5], q4), tr2(vf[6], vf[7], q4)};
    const v4u a1 = {tr2(vf[0], vf[1], q4 + 4), tr2(vf[2], vf[3], q4 + 4), tr2(vf[4], vf[5], q4 + 4), tr2(vf[6], vf[7], q4 + 4)};
    acc[q4] = mfma32(a1, v4u{ph[0], ph[1], ph[2], ph[3]}, mfma32(a0, v4u{pl[0], pl[1], pl[2], pl[3]}, f32x4{0.f, 0.f, 0.f, 0.f}));
  }
#pragma unroll
  for (int n = XPT + 7; n < 20; ++n) PV_(n);
  const __amdgpu_buffer_rsrc_t rwo = MKRS(br ? woB : woA, 8 << 20);
  const int wop = (128 * h + 32 * (w >> 1) + 4 * (w & 1) + g) * 2048 + 512 * cq + rho * 16;
#define WLOAD(n) LDB(rwo, wop + ((n) >> 1) * (8 * 2048) + ((n) & 1) * 256, W_AUX)
#define PW(n) do { SB(); waitvm<WIN - 1>(); wo[n] = WLOAD(n); SB(); } while (0)
  SB(); wo[0] = WLOAD(0); SB();
  STAMP(14);
#pragma unroll
  for (int q4 = 0; q4 < 4; ++q4) {
    const v4u a0 = {tr2(vf[8], vf[9], q4), tr2(vf[10], vf[11], q4), tr2(vf[12], vf[13], q4), tr2(vf[14], vf[15], q4)};
    const v4u a1 = {tr2(vf[8], vf[9], q4 + 4), tr2(vf[10], vf[11], q4 + 4), tr2(vf[12], vf[13], q4 + 4), tr2(vf[14], vf[15], q4 + 4)};
    acc[q4] = mfma32(a1, v4u{ph[4], ph[5], ph[6], ph[7]}, mfma32(a0, v4u{pl[4], pl[5], pl[6], pl[7]}, acc[q4]));
  }
  LAS f32x4* dA = (LAS f32x4*)(L + L_RED + (w * 8 + (l & 7)) * 528 + g * 128 + (rho >> 3) * 64);
#pragma unroll
  for (int q4 = 0; q4 < 4; ++q4) dA[q4] = acc[q4];
  PW(1); PW(2); PW(3);
  STAMP(20);
#pragma unroll
  for (int q4 = 0; q4 < 4; ++q4) {
    const v2u a0 = {tr2(vf[16], vf[17], q4), tr2(vf[18], vf[19], q4)};
    const v2u a1 = {tr2(vf[16], vf[17], q4 + 4), tr2(vf[18], vf[19], q4 + 4)};
    dA[8 * 8 * 33 + q4] = mfma16(a1, v2u{ph[8], ph[9]}, mfma16(a0, v2u{pl[8], pl[9]}, f32x4{0.f, 0.f, 0.f, 0.f}));
  }
  SB(); wo[4] = WLOAD(4); SB();
  STAMP(6);
  lds_barrier();
  STAMP(15);
  {
    const int pb = tid >> 8, hd = (tid >> 5) & 7, quad = tid & 31;
    const LAS char* src = L + L_RED + pb * (8 * 8 * 528) + hd * 528 + quad * 16;
    f32x4 a = *(const LAS f32x4*)src;
#pragma unroll
    for (int ww = 1; ww < 8; ++ww) a += *(const LAS f32x4*)(src + ww * (8 * 528));
    const int rec = (pb * 32 + kvh * 8 + hd) * 64 + b * 16 + s;
    __builtin_amdgcn_raw_buffer_store_b128(nosent(a), rs, WS_PO + rec * 512 + quad * 16, 0, AUX_SC1);
  }
  STAMP(7);
  SB();
#pragma unroll
  for (int n = 5; n < 8; ++n) wo[n] = WLOAD(n);
  SB();
  v2u wt[16];
#pragma unroll
  for (int m = 0; m < 16; ++m) {
    wt[m] = v2u{tr2(wo[m >> 3], wo[2 + (m >> 3)], m & 7), tr2(wo[4 + (m >> 3)], wo[6 + (m >> 3)], m & 7)};
    asm volatile("" : "+v"(wt[m]));
  }
  const int sg = (l >> 2) & 3;
  STAMP(21);
  SB();

  {
    const int po = WS_PO + ((br * 32 + h) * 64 + (l & 3) * 16 + sg) * 512 + (16 * w + 4 * g) * 4;
    v4u pv[4];
#pragma unroll
    for (int n = 0; n < 4; ++n) pv[n] = __builtin_amdgcn_raw_buffer_load_b128(rs, po + n * 2048, 0, AUX_SC1);
    SB();
    lds_wait(L, S_W, 1);
    const f32x4 wg = *(const LAS f32x4*)(L + L_WGT + ((l & 3) * 16 + sg * 4) * 4);
    SB();
    for (unsigned spin = 0;; ++spin) {
      const bool bad = has_sent(pv[0]) | has_sent(pv[1]) | has_sent(pv[2]) | has_sent(pv[3]);
      if (__builtin_amdgcn_ballot_w64(bad) == 0ull || spin > SPIN_CAP) break;
      asm volatile("" ::: "memory");
      if (bad) {
#pragma unroll
        for (int n = 0; n < 4; ++n) pv[n] = __builtin_amdgcn_raw_buffer_load_b128(rs, po + n * 2048, 0, AUX_SC1);
      }
    }
    STAMP(8);
    f32x4 o = __builtin_bit_cast(f32x4, pv[0]) * wg.x + __builtin_bit_cast(f32x4, pv[1]) * wg.y +
              (__builtin_bit_cast(f32x4, pv[2]) * wg.z + __builtin_bit_cast(f32x4, pv[3]) * wg.w);
#pragma unroll
    for (int k = 0; k < 4; ++k) { float t = o[k]; t += dpp<ROR8>(t); t += dpp<ROR4>(t); o[k] = t; }
    const unsigned o01 = pk_bf16(o.x, o.y), o23 = pk_bf16(o.z, o.w);
    v2u bm[4];
#pragma unroll
    for (int k = 0; k < 4; ++k) bm[k] = v2u{sg == k ? o01 : 0u, sg == k ? o23 : 0u};
    LAS f32x4* d = (LAS f32x4*)(L + L_RED2 + (w * 4 + (l & 3)) * 1056 + g * 128 + sg * 16);
#pragma unroll
    for (int a = 0; a < 4; ++a) {
      f32x4 z = {0.f, 0.f, 0.f, 0.f};
#pragma unroll
      for (int k = 0; k < 4; ++k) z = mfma16(wt[4 * a + k], bm[k], z);
      d[(a >> 1) * 32 + (a & 1) * 4] = z;
    }
  }
  STAMP(18);
  lds_barrier();
  if (tid < 256) {
    const int bb = tid >> 6, quad = tid & 63;
    const LAS char* src = L + L_RED2 + bb * 1056 + quad * 16;
    f32x4 a = *(const LAS f32x4*)src;
#pragma unroll
    for (int ww = 1; ww < 8; ++ww) a += *(const LAS f32x4*)(src + ww * (4 * 1056));
    const int cons = (br * 4 + cq) * 32 + bb * 8 + (quad >> 3);
    __builtin_amdgcn_raw_buffer_store_b128(nosent(a), rs, WS_OUT + cons * 4096 + h * 128 + (quad & 7) * 16, 0, AUX_SC1);
  }
  STAMP(9);
  if (w == 7) {
    const int io = WS_OUT + idx * 4096 + l * 16;
    v4u pv[4];
    for (unsigned spin = 0;; ++spin) {
      asm volatile("" ::: "memory");
#pragma unroll
      for (int n = 0; n < 4; ++n) pv[n] = __builtin_amdgcn_raw_buffer_load_b128(rs, io + n * 1024, 0, AUX_SC1);
      const bool bad = has_sent(pv[0]) | has_sent(pv[1]) | has_sent(pv[2]) | has_sent(pv[3]);
      if (__builtin_amdgcn_ballot_w64(bad) == 0ull || spin > SPIN_CAP) break;
      __builtin_amdgcn_s_sleep(1);
    }
    f32x4 a = (__builtin_bit_cast(f32x4, pv[0]) + __builtin_bit_cast(f32x4, pv[1])) +
              (__builtin_bit_cast(f32x4, pv[2]) + __builtin_bit_cast(f32x4, pv[3]));
#pragma unroll
    for (int k = 0; k < 4; ++k) { float t = a[k]; t += dpp<ROR8>(t); a[k] = x32_sum(x16_sum(t)); }
    const int r = (l >> 3) & 3;
    const float v = r == 0 ? a.x : r == 1 ? a.y : r == 2 ? a.z : a.w;
    const int obr = idx >> 7, ocq = (idx >> 5) & 3, ob = (idx >> 3) & 3, oc = idx & 7;
    if (l < 32) ((GAS unsigned short*)out)[ob * 2048 + obr * 1024 + ocq * 256 + oc * 32 + l] = (unsigned short)(pk_bf16(v, v) & 0xFFFFu);
    STAMP(10);
  }
  STAMP(22);
  STAMP_FLUSH();
}

extern "C" void kernel_launch(void* const* d_in, const int* in_sizes, int n_in, void* d_out, int out_size, void* d_ws,
                              size_t ws_size, hipStream_t stream) {
  hipLaunchKernelGGL(fused_decode, dim3(256), dim3(576), 0, stream, (const char*)d_in[0], (const char*)d_in[1],
                     (const char*)d_in[2], (const char*)d_in[3], (const char*)d_in[4], (const char*)d_in[5],
                     (const char*)d_in[6], (const char*)d_in[7], (const char*)d_in[8], (const char*)d_in[9],
                     (unsigned short*)d_out, (char*)d_ws);
}
```

```cpp
#define DIAG 1
#include <hip/hip_runtime.h>
#include <stdint.h>

#define KV_AUX 2
#define W_AUX 2
#define WIN 4
#define XPT 8
#define PRETOUCH 1
#define STAMPS 0

typedef unsigned v4u __attribute__((ext_vector_type(4)));
typedef unsigned v2u __attribute__((ext_vector_type(2)));
typedef float f32x4 __attribute__((ext_vector_type(4)));
typedef __bf16 bf16x8 __attribute__((ext_vector_type(8)));
typedef short s16x4 __attribute__((ext_vector_type(4)));
#define GAS __attribute__((address_space(1)))
#define LAS __attribute__((address_space(3)))
#define SB() __builtin_amdgcn_sched_barrier(0)

constexpr int WS_EPOCH = 0;
constexpr int WS_FLAG = 4096;
constexpr int WS_QP = WS_FLAG + 256 * 128;
constexpr int WS_ML = WS_QP + 2 * 4 * 4 * 2 * 1024 * 4;
constexpr int WS_PO = WS_ML + 256 * 128;
constexpr int WS_OUT = WS_PO + 2 * 32 * 64 * 128 * 4;
constexpr int WS_END = WS_OUT + 256 * 32 * 32 * 4;
constexpr int WS_STAMP = WS_END;
constexpr int AUX_SC1 = 16;
constexpr unsigned SENT = 0xFFFFFFFFu;
constexpr unsigned SPIN_CAP = 1u << 16;

constexpr int L_SYNC = 0;
constexpr int L_WGT = 64;
constexpr int L_QRED = 512;
constexpr int L_QT = L_QRED + 16384;
constexpr int L_QS = L_QT + 1024;
constexpr int L_MX = L_QS + 2 * 8 * 272;
constexpr int L_LS = L_MX + 512;
constexpr int L_RED = L_LS + 512;
constexpr int L_RED2 = L_RED + 2 * 8 * 8 * 528;
constexpr int L_END = L_RED2 + 8 * 4 * 1056;
constexpr int S_QP = 0, S_Q = 4, S_MX = 8, S_LS = 12, S_W = 16;

#define MKRS(ptr, n) __builtin_amdgcn_make_buffer_rsrc((void*)(ptr), 0, (n), 0x00020000)
#define LDB(rs, off, aux) __builtin_amdgcn_raw_buffer_load_b128((rs), (off), 0, (aux))
__device__ __forceinline__ f32x4 mfma32(v4u a, v4u b, f32x4 c) {
  return __builtin_amdgcn_mfma_f32_16x16x32_bf16(__builtin_bit_cast(bf16x8, a), __builtin_bit_cast(bf16x8, b), c, 0, 0, 0);
}
__device__ __forceinline__ f32x4 mfma16(v2u a, v2u b, f32x4 c) {
  return __builtin_amdgcn_mfma_f32_16x16x16bf16_1k(__builtin_bit_cast(s16x4, a), __builtin_bit_cast(s16x4, b), c, 0, 0, 0);
}
__device__ __forceinline__ unsigned pk_bf16(float lo, float hi) {
  typedef __bf16 bf16x2 __attribute__((ext_vector_type(2)));
  typedef float f32x2 __attribute__((ext_vector_type(2)));
  f32x2 v = {lo, hi};
  return __builtin_bit_cast(unsigned, __builtin_convertvector(v, bf16x2));
}
__device__ __forceinline__ unsigned tr2(const v4u& r0, const v4u& r1, int dd) {
  return __builtin_amdgcn_perm(r1[dd >> 1], r0[dd >> 1], (dd & 1) ? 0x07060302u : 0x05040100u);
}
__device__ __forceinline__ float xg_max(float x) {
  auto s = __builtin_amdgcn_permlane16_swap(__float_as_uint(x), __float_as_uint(x), false, false);
  x = fmaxf(__uint_as_float(s[0]), __uint_as_float(s[1]));
  auto t = __builtin_amdgcn_permlane32_swap(__float_as_uint(x), __float_as_uint(x), false, false);
  return fmaxf(__uint_as_float(t[0]), __uint_as_float(t[1]));
}
__device__ __forceinline__ float x16_sum(float x) {
  auto s = __builtin_amdgcn_permlane16_swap(__float_as_uint(x), __float_as_uint(x), false, false);
  return __uint_as_float(s[0]) + __uint_as_float(s[1]);
}
__device__ __forceinline__ float x32_sum(float x) {
  auto t = __builtin_amdgcn_permlane32_swap(__float_as_uint(x), __float_as_uint(x), false, false);
  return __uint_as_float(t[0]) + __uint_as_float(t[1]);
}
template <int CTRL> __device__ __forceinline__ float dpp(float x) {
  return __builtin_bit_cast(float, __builtin_amdgcn_mov_dpp(__builtin_bit_cast(int, x), CTRL, 0xf, 0xf, true));
}
constexpr int ROR1 = 0x121, ROR2 = 0x122, ROR4 = 0x124, ROR8 = 0x128;
__device__ __forceinline__ v4u nosent(f32x4 v) {
  v4u u = __builtin_bit_cast(v4u, v);
  u.x = min(u.x, SENT - 1u); u.y = min(u.y, SENT - 1u); u.z = min(u.z, SENT - 1u); u.w = min(u.w, SENT - 1u);
  return u;
}
__device__ __forceinline__ bool has_sent(v4u u) { return max(max(u.x, u.y), max(u.z, u.w)) == SENT; }
__device__ __forceinline__ void lds_barrier() {
  asm volatile("s_waitcnt lgkmcnt(0)\n\ts_barrier" ::: "memory");
}
template <int N> __device__ __forceinline__ void waitvm() { asm volatile("s_waitcnt vmcnt(%0)" ::"i"(N) : "memory"); }
__device__ __forceinline__ void lds_signal(LAS char* L, int word, int l) {
  asm volatile("s_waitcnt lgkmcnt(0)" ::: "memory");
  if (l == 0) __hip_atomic_fetch_add((LAS unsigned*)(L + L_SYNC + word), 1u, __ATOMIC_RELAXED, __HIP_MEMORY_SCOPE_WORKGROUP);
  asm volatile("" ::: "memory");
}
__device__ __forceinline__ void lds_wait(LAS char* L, int word, unsigned want) {
  for (;;) {
    const unsigned v = __hip_atomic_load((LAS unsigned*)(L + L_SYNC + word), __ATOMIC_RELAXED, __HIP_MEMORY_SCOPE_WORKGROUP);
    if (__builtin_amdgcn_readfirstlane(v) >= want) break;
    __builtin_amdgcn_s_sleep(1);
  }
  asm volatile("" ::: "memory");
}

#define STAMP(n) do { if ((n) == 0 || (n) == DIAG) { SB(); ts[(n) == 0 ? 0 : 1] = __builtin_amdgcn_s_memrealtime(); SB(); } } while (0)
#define STAMP_FLUSH() do { if (w == 7) { const unsigned long long tgt = (DIAG ? ts[1] : ts[0]) + 4000ull; while (__builtin_amdgcn_s_memrealtime() < tgt) __builtin_amdgcn_s_sleep(2); } } while (0)

__global__ void __launch_bounds__(576, 3)
fused_decode(const char* __restrict__ xA, const char* __restrict__ kA, const char* __restrict__ vA, const char* __restrict__ wqA,
             const char* __restrict__ woA, const char* __restrict__ xB, const char* __restrict__ kB, const char* __restrict__ vB,
             const char* __restrict__ wqB, const char* __restrict__ woB, unsigned short* __restrict__ out, char* __restrict__ ws) {
  __shared__ __attribute__((aligned(16))) char smem[L_END];
  LAS char* L = (LAS char*)smem;
  const int c = blockIdx.x, tid = threadIdx.x, l = tid & 63, w = __builtin_amdgcn_readfirstlane(tid >> 6);
  const int idx = (c & 7) * 32 + (c >> 3), kvh = idx >> 6, gi = idx & 63;
  const int br = gi >> 5, cb = gi & 31;
  const int b = gi >> 4, s = gi & 15;
  const int h = kvh * 8 + ((gi >> 2) & 7), cq = gi & 3;
  const int g = l >> 4, rho = l & 15;
  const __amdgpu_buffer_rsrc_t rs = __builtin_amdgcn_make_buffer_rsrc((void*)ws, 0, WS_END, 0x00020000);
  unsigned long long ts[2] = {0, 0};
  STAMP(0);

  constexpr float C2 = 0.08838834764831845f * 1.4426950408889634f;

  if (w == 8) {
    __builtin_amdgcn_s_setprio(3);
    const unsigned long long epoch =
        __hip_atomic_load((GAS unsigned long long*)(ws + WS_EPOCH), __ATOMIC_RELAXED, __HIP_MEMORY_SCOPE_AGENT);
    if (l < 16) *(LAS unsigned*)(L + L_SYNC + l * 4) = 0u;
    unsigned touch = 0;
    {
      const size_t cub = (size_t)(b * 4 + kvh) * (8192 * 256) + (size_t)(128 * s) * 256;
      const int t = l & 15;
      const char* p = t < 4 ? kA + cub + t * (2048 * 256) : t < 8 ? vA + cub + (t - 4) * (2048 * 256)
                    : t == 8 ? kB + cub + 6144 * 256 : t == 9 ? vB + cub + 6144 * 256
                    : t < 14 ? (br ? woB : woA) + (size_t)(128 * h + 32 * (t - 10)) * 2048 + 512 * cq
                    : t == 14 ? (const char*)out : ws + WS_OUT + ((br * 4 + cq) * 32) * 4096;
      if (l < 16) touch = __builtin_nontemporal_load((const GAS unsigned*)p);
    }
    const int qslot = WS_QP + ((((br * 4 + kvh) * 4 + (l >> 4)) * 2 + (cb & 1)) * 1024 + (cb >> 1) * 64 + (l & 15) * 4) * 4;
    {
      const v4u sent = {SENT, SENT, SENT, SENT};
      __builtin_amdgcn_raw_buffer_store_b128(sent, rs, qslot, 0, AUX_SC1);
      const int po = WS_PO + ((br * 32 + h) * 64 + cq * 16) * 512 + l * 16;
#pragma unroll
      for (int n = 0; n < 8; ++n) __builtin_amdgcn_raw_buffer_store_b128(sent, rs, po + n * 1024, 0, AUX_SC1);
      const int oo = WS_OUT + idx * 4096 + l * 16;
#pragma unroll
      for (int n = 0; n < 4; ++n) __builtin_amdgcn_raw_buffer_store_b128(sent, rs, oo + n * 1024, 0, AUX_SC1);
      if (l < 8) __builtin_amdgcn_raw_buffer_store_b128(sent, rs, WS_ML + idx * 128 + l * 16, 0, AUX_SC1);
    }
    lds_barrier();
    const unsigned long long tag = epoch + 1ull;
    const unsigned tlo = (unsigned)tag, thi = (unsigned)(tag >> 32);
    waitvm<0>();
    asm volatile("" ::"v"(touch));
    if (l == 0) __builtin_amdgcn_raw_buffer_store_b64(v2u{tlo, thi}, rs, WS_FLAG + idx * 128, 0, AUX_SC1);
    STAMP(2);
    lds_wait(L, S_QP, 8);
    STAMP(1);
    {
      const LAS f32x4* src = (const LAS f32x4*)(L + L_QRED) + l;
      f32x4 a = src[0];
#pragma unroll
      for (int n = 1; n < 16; ++n) a += src[n * 64];
      LAS float* t = (LAS float*)(L + L_QT) + (l >> 3) * 32 + (l & 7);
      t[0] = a.x; t[8] = a.y; t[16] = a.z; t[24] = a.w;
      asm volatile("s_waitcnt lgkmcnt(0)" ::: "memory");
      a = *((const LAS f32x4*)(L + L_QT) + l);
      __builtin_amdgcn_raw_buffer_store_b128(nosent(a), rs, qslot, 0, AUX_SC1);
    }
    STAMP(16);
    {
      const int f1 = WS_FLAG + (kvh * 64 + l) * 128, f2 = WS_FLAG + ((br * 4 + cq) * 32 + (l & 31)) * 128;
      for (unsigned spin = 0;; ++spin) {
        asm volatile("" ::: "memory");
        const v2u x1 = __builtin_amdgcn_raw_buffer_load_b64(rs, f1, 0, AUX_SC1);
        const v2u x2 = __builtin_amdgcn_raw_buffer_load_b64(rs, f2, 0, AUX_SC1);
        if (__builtin_amdgcn_ballot_w64(((x1.x ^ tlo) | (x1.y ^ thi) | (x2.x ^ tlo) | (x2.y ^ thi)) != 0u) == 0ull || spin > SPIN_CAP) break;
        __builtin_amdgcn_s_sleep(8);
      }
      asm volatile("" ::: "memory");
    }
    STAMP(19);
    {
      const int qo = WS_QP + ((kvh * 4 + b) * 2 * 1024) * 4 + l * 16;
      v4u qg[16];
#pragma unroll
      for (int n = 0; n < 16; ++n) qg[n] = v4u{SENT, SENT, SENT, SENT};
      for (unsigned spin = 0;; ++spin) {
        asm volatile("" ::: "memory");
#pragma unroll
        for (int n = 0; n < 16; ++n)
          if (has_sent(qg[n])) qg[n] = __builtin_amdgcn_raw_buffer_load_b128(rs, qo + (n & 7) * 1024, (n >> 3) * (16 * 2 * 1024 * 4), AUX_SC1);
        bool bad = false;
#pragma unroll
        for (int n = 0; n < 16; ++n) bad |= has_sent(qg[n]);
        if (__builtin_amdgcn_ballot_w64(bad) == 0ull || spin > SPIN_CAP) break;
        __builtin_amdgcn_s_sleep(4);
      }
#pragma unroll
      for (int n = 0; n < 8; ++n) {
        const f32x4 q4 = __builtin_bit_cast(f32x4, qg[(n >> 2) * 8 + (n & 3)]) + __builtin_bit_cast(f32x4, qg[(n >> 2) * 8 + 4 + (n & 3)]);
        *(LAS v2u*)(L + L_QS + (2 * n + (l >> 5)) * 272 + (l & 31) * 8) = v2u{pk_bf16(q4.x, q4.y), pk_bf16(q4.z, q4.w)};
      }
    }
    lds_signal(L, S_Q, l);
    STAMP(3);
    if (idx == 0) {
      for (int pass = 0; pass < 4; ++pass)
        for (unsigned spin = 0;; ++spin) {
          asm volatile("" ::: "memory");
          const v2u x2 = __builtin_amdgcn_raw_buffer_load_b64(rs, WS_FLAG + (pass * 64 + l) * 128, 0, AUX_SC1);
          if (__builtin_amdgcn_ballot_w64(((x2.x ^ tlo) | (x2.y ^ thi)) != 0u) == 0ull || spin > SPIN_CAP) break;
          __builtin_amdgcn_s_sleep(8);
        }
      if (l == 0) __hip_atomic_store((GAS unsigned long long*)(ws + WS_EPOCH), tag, __ATOMIC_RELAXED, __HIP_MEMORY_SCOPE_AGENT);
    }
    lds_wait(L, S_LS, 8);
    if (l < 16) {
      const LAS f32x4* ls = (const LAS f32x4*)(L + L_LS + l * 32);
      const LAS f32x4* mx = (const LAS f32x4*)(L + L_MX + l * 32);
      const f32x4 l0 = ls[0], l1 = ls[1], m0 = mx[0], m1 = mx[1];
      const float lsum = ((l0.x + l0.y) + (l0.z + l0.w)) + ((l1.x + l1.y) + (l1.z + l1.w));
      const float mm = fmaxf(fmaxf(fmaxf(m0.x, m0.y), fmaxf(m0.z, m0.w)), fmaxf(fmaxf(m1.x, m1.y), fmaxf(m1.z, m1.w))) * C2;
      __builtin_amdgcn_raw_buffer_store_b64(v2u{min(__float_as_uint(mm), SENT - 1u), min(__float_as_uint(lsum), SENT - 1u)}, rs,
                                            WS_ML + idx * 128 + l * 8, 0, AUX_SC1);
    }
    STAMP(5);
    lds_barrier();
    {
      const int mo = WS_ML + (kvh * 64 + l) * 128 + (br * 8 + (h & 7)) * 8;
      v2u ml = {SENT, SENT};
      for (unsigned spin = 0;; ++spin) {
        asm volatile("" ::: "memory");
        if (max(ml.x, ml.y) == SENT) ml = __builtin_amdgcn_raw_buffer_load_b64(rs, mo, 0, AUX_SC1);
        if (__builtin_amdgcn_ballot_w64(max(ml.x, ml.y) == SENT) == 0ull || spin > SPIN_CAP) break;
        __builtin_amdgcn_s_sleep(4);
      }
      const float m = __uint_as_float(ml.x), ls = __uint_as_float(ml.y);
      float M = m;
      M = fmaxf(M, dpp<ROR8>(M)); M = fmaxf(M, dpp<ROR4>(M)); M = fmaxf(M, dpp<ROR2>(M)); M = fmaxf(M, dpp<ROR1>(M));
      const float e = __builtin_amdgcn_exp2f(m - M);
      float Ls = e * ls;
      Ls += dpp<ROR8>(Ls); Ls += dpp<ROR4>(Ls); Ls += dpp<ROR2>(Ls); Ls += dpp<ROR1>(Ls);
      *(LAS float*)(L + L_WGT + ((l >> 4) * 16 + (l & 3) * 4 + ((l >> 2) & 3)) * 4) = e / Ls;
    }
    lds_signal(L, S_W, l);
    STAMP(12);
    STAMP(22);
    STAMP_FLUSH();
    return;
  }

  const int qrow = 512 * (cb & 1) + 64 * w;
  const v4u xf = *(const GAS v4u*)((br ? xB : xA) + ((l & 3) * 1024 + qrow + 16 * g + 8 * ((l >> 3) & 1)) * 2);
  v4u wr[8];
  const __amdgpu_buffer_rsrc_t rwq = MKRS(br ? wqB : wqA, 8 << 20);
  const int wqp = (qrow + 8 * (l >> 3)) * 8192 + (kvh * 1024 + (cb >> 1) * 64) * 2 + (l & 7) * 16;
#pragma unroll
  for (int n = 0; n < WIN; ++n) wr[n] = LDB(rwq, wqp + n * 8192, W_AUX);
  SB();
  asm volatile("s_barrier" ::: "memory");
  SB();
#pragma unroll
  for (int n = WIN; n < 8; ++n) { waitvm<WIN - 1>(); wr[n] = LDB(rwq, wqp + n * 8192, W_AUX); SB(); }
  const int wbase = (b * 4 + kvh) * (8192 * 256) + (128 * s + 16 * w) * 256;
  const int koff = wbase + (4 * (rho & 3) + ((rho >> 2) & 1)) * 256 + g * 16 + 64 * (rho >> 3);
  const int voff = wbase + l * 16;
  constexpr int TS = 2048 * 256;
  v4u kf[20], vf[20], wo[8];
  const __amdgpu_buffer_rsrc_t rkA = MKRS(kA, 32 << 20), rkB = MKRS(kB, 32 << 20), rvA = MKRS(vA, 32 << 20), rvB = MKRS(vB, 32 << 20);
#define KLOAD(n) __builtin_amdgcn_raw_buffer_load_b128((n) < 16 ? rkA : rkB, koff + ((n) & 1) * 128 + (((n) >> 1) & 1) * 512, ((n) < 16 ? ((n) >> 2) : 3) * TS, KV_AUX)
#define VLOAD(n) __builtin_amdgcn_raw_buffer_load_b128((n) < 16 ? rvA : rvB, voff + ((n) & 3) * 1024, ((n) < 16 ? ((n) >> 2) : 3) * TS, KV_AUX)
#define PK(n) do { SB(); waitvm<WIN - 1>(); kf[n] = KLOAD(n); SB(); } while (0)
#define PV_(n) do { SB(); waitvm<WIN - 1>(); vf[n] = VLOAD(n); SB(); } while (0)
#define PVX(n) do { SB(); vf[n] = VLOAD(n); SB(); } while (0)
#pragma unroll
  for (int n = 0; n < WIN; ++n) PK(n);
  SB(); kf[WIN] = KLOAD(WIN); SB();

  {
    f32x4 qa[8];
#pragma unroll
    for (int dd = 0; dd < 8; ++dd) {
      const v4u a = {tr2(wr[0], wr[1], dd), tr2(wr[2], wr[3], dd), tr2(wr[4], wr[5], dd), tr2(wr[6], wr[7], dd)};
      qa[dd] = mfma32(a, xf, f32x4{0.f, 0.f, 0.f, 0.f});
    }
    if ((l & 12) == ((g >> 1) << 3)) {
      LAS f32x4* d = (LAS f32x4*)(L + L_QRED + ((w * 2 + (g >> 1)) * 4 + (l & 3)) * 256 + (g & 1) * 128);
#pragma unroll
      for (int dd = 0; dd < 8; ++dd) d[dd] = qa[dd];
    }
  }
  lds_signal(L, S_QP, l);
  STAMP(1);
#pragma unroll
  for (int n = WIN + 1; n < 20; ++n) PK(n);
#pragma unroll
  for (int n = 0; n < XPT; ++n) PV_(n);
  STAMP(4);
  lds_wait(L, S_Q, 1);
  STAMP(11);
  PVX(XPT);

  f32x4 sc[5];
  {
    const LAS char* qp = L + L_QS + (l & 7) * 272 + g * 16 + 64 * (rho >> 3);
    const v4u qa0 = *(const LAS v4u*)qp, qa1 = *(const LAS v4u*)(qp + 128);
    const v4u qb0 = *(const LAS v4u*)(qp + 8 * 272), qb1 = *(const LAS v4u*)(qp + 8 * 272 + 128);
#pragma unroll
    for (int u = 0; u < 5; ++u) {
      const f32x4 z = {0.f, 0.f, 0.f, 0.f};
      const f32x4 t1 = mfma32(kf[4 * u + 1], u < 4 ? qa1 : qb1, mfma32(kf[4 * u], u < 4 ? qa0 : qb0, z));
      const f32x4 t2 = mfma32(kf[4 * u + 3], u < 4 ? qa1 : qb1, mfma32(kf[4 * u + 2], u < 4 ? qa0 : qb0, z));
#pragma unroll
      for (int r = 0; r < 4; ++r) {
        auto sw = __builtin_amdgcn_permlane32_swap(__float_as_uint(t1[r]), __float_as_uint(t2[r]), false, false);
        sc[u][r] = __uint_as_float(sw[0]) + dpp<ROR8>(__uint_as_float(sw[1]));
      }
      if (u == 2) PVX(XPT + 1);
    }
  }
  {
    float mB = fmaxf(fmaxf(sc[4].x, sc[4].y), fmaxf(sc[4].z, sc[4].w));
    float mA = fmaxf(fmaxf(sc[0].x, sc[0].y), fmaxf(sc[0].z, sc[0].w));
#pragma unroll
    for (int u = 1; u < 4; ++u) mA = fmaxf(mA, fmaxf(fmaxf(sc[u].x, sc[u].y), fmaxf(sc[u].z, sc[u].w)));
    mA = xg_max(mA); mB = xg_max(mB);
    if (l < 8) {
      *(LAS float*)(L + L_MX + (l * 8 + w) * 4) = mA;
      *(LAS float*)(L + L_MX + 256 + (l * 8 + w) * 4) = mB;
    }
  }
  lds_signal(L, S_MX, l);
  STAMP(5);
  PV_(XPT + 2);
  PV_(XPT + 3);
  lds_wait(L, S_MX, 8);
  STAMP(12);
  PVX(XPT + 4);
  float MA, MB;
  {
    const LAS f32x4* m = (const LAS f32x4*)(L + L_MX + (l & 7) * 32);
    const f32x4 a0 = m[0], a1 = m[1], b0 = m[16], b1 = m[17];
    MA = fmaxf(fmaxf(fmaxf(a0.x, a0.y), fmaxf(a0.z, a0.w)), fmaxf(fmaxf(a1.x, a1.y), fmaxf(a1.z, a1.w))) * C2;
    MB = fmaxf(fmaxf(fmaxf(b0.x, b0.y), fmaxf(b0.z, b0.w)), fmaxf(fmaxf(b1.x, b1.y), fmaxf(b1.z, b1.w))) * C2;
  }
  unsigned pl[10], ph[10];
  {
    float la = 0.f, lb = 0.f;
#pragma unroll
    for (int u = 0; u < 5; ++u) {
      const float M = u < 4 ? MA : MB;
      const float p0 = __builtin_amdgcn_exp2f(fmaf(sc[u].x, C2, -M)), p1 = __builtin_amdgcn_exp2f(fmaf(sc[u].y, C2, -M));
      const float p2 = __builtin_amdgcn_exp2f(fmaf(sc[u].z, C2, -M)), p3 = __builtin_amdgcn_exp2f(fmaf(sc[u].w, C2, -M));
      const float t = (p0 + p1) + (p2 + p3);
      if (u < 4) la += t; else lb = t;
      pl[2 * u] = rho < 8 ? pk_bf16(p0, p1) : 0u; pl[2 * u + 1] = rho < 8 ? pk_bf16(p2, p3) : 0u;
      ph[2 * u] = __builtin_amdgcn_mov_dpp(pl[2 * u], ROR8, 0xf, 0xf, true);
      ph[2 * u + 1] = __builtin_amdgcn_mov_dpp(pl[2 * u + 1], ROR8, 0xf, 0xf, true);
      if (u == 2) PVX(XPT + 5);
    }
    la = x32_sum(x16_sum(la)); lb = x32_sum(x16_sum(lb));
    if (l < 8) {
      *(LAS float*)(L + L_LS + (l * 8 + w) * 4) = la;
      *(LAS float*)(L + L_LS + 256 + (l * 8 + w) * 4) = lb;
    }
  }
  lds_signal(L, S_LS, l);
  STAMP(13);
  PV_(XPT + 6);
  f32x4 acc[4];
#pragma unroll
  for (int q4 = 0; q4 < 4; ++q4) {
    const v4u a0 = {tr2(vf[0], vf[1], q4), tr2(vf[2], vf[3], q4), tr2(vf[4], vf[5], q4), tr2(vf[6], vf[7], q4)};
    const v4u a1 = {tr2(vf[0], vf[1], q4 + 4), tr2(vf[2], vf[3], q4 + 4), tr2(vf[4], vf[5], q4 + 4), tr2(vf[6], vf[7], q4 + 4)};
    acc[q4] = mfma32(a1, v4u{ph[0], ph[1], ph[2], ph[3]}, mfma32(a0, v4u{pl[0], pl[1], pl[2], pl[3]}, f32x4{0.f, 0.f, 0.f, 0.f}));
  }
#pragma unroll
  for (int n = XPT + 7; n < 20; ++n) PV_(n);
  const __amdgpu_buffer_rsrc_t rwo = MKRS(br ? woB : woA, 8 << 20);
  const int wop = (128 * h + 32 * (w >> 1) + 4 * (w & 1) + g) * 2048 + 512 * cq + rho * 16;
#define WLOAD(n) LDB(rwo, wop + ((n) >> 1) * (8 * 2048) + ((n) & 1) * 256, W_AUX)
#define PW(n) do { SB(); waitvm<WIN - 1>(); wo[n] = WLOAD(n); SB(); } while (0)
  SB(); wo[0] = WLOAD(0); SB();
  STAMP(14);
#pragma unroll
  for (int q4 = 0; q4 < 4; ++q4) {
    const v4u a0 = {tr2(vf[8], vf[9], q4), tr2(vf[10], vf[11], q4), tr2(vf[12], vf[13], q4), tr2(vf[14], vf[15], q4)};
    const v4u a1 = {tr2(vf[8], vf[9], q4 + 4), tr2(vf[10], vf[11], q4 + 4), tr2(vf[12], vf[13], q4 + 4), tr2(vf[14], vf[15], q4 + 4)};
    acc[q4] = mfma32(a1, v4u{ph[4], ph[5], ph[6], ph[7]}, mfma32(a0, v4u{pl[4], pl[5], pl[6], pl[7]}, acc[q4]));
  }
  LAS f32x4* dA = (LAS f32x4*)(L + L_RED + (w * 8 + (l & 7)) * 528 + g * 128 + (rho >> 3) * 64);
#pragma unroll
  for (int q4 = 0; q4 < 4; ++q4) dA[q4] = acc[q4];
  PW(1); PW(2); PW(3);
  STAMP(20);
#pragma unroll
  for (int q4 = 0; q4 < 4; ++q4) {
    const v2u a0 = {tr2(vf[16], vf[17], q4), tr2(vf[18], vf[19], q4)};
    const v2u a1 = {tr2(vf[16], vf[17], q4 + 4), tr2(vf[18], vf[19], q4 + 4)};
    dA[8 * 8 * 33 + q4] = mfma16(a1, v2u{ph[8], ph[9]}, mfma16(a0, v2u{pl[8], pl[9]}, f32x4{0.f, 0.f, 0.f, 0.f}));
  }
  SB(); wo[4] = WLOAD(4); SB();
  STAMP(6);
  lds_barrier();
  STAMP(15);
  {
    const int pb = tid >> 8, hd = (tid >> 5) & 7, quad = tid & 31;
    const LAS char* src = L + L_RED + pb * (8 * 8 * 528) + hd * 528 + quad * 16;
    f32x4 a = *(const LAS f32x4*)src;
#pragma unroll
    for (int ww = 1; ww < 8; ++ww) a += *(const LAS f32x4*)(src + ww * (8 * 528));
    const int rec = (pb * 32 + kvh * 8 + hd) * 64 + b * 16 + s;
    __builtin_amdgcn_raw_buffer_store_b128(nosent(a), rs, WS_PO + rec * 512 + quad * 16, 0, AUX_SC1);
  }
  STAMP(7);
  SB();
#pragma unroll
  for (int n = 5; n < 8; ++n) wo[n] = WLOAD(n);
  SB();
  v2u wt[16];
#pragma unroll
  for (int m = 0; m < 16; ++m) {
    wt[m] = v2u{tr2(wo[m >> 3], wo[2 + (m >> 3)], m & 7), tr2(wo[4 + (m >> 3)], wo[6 + (m >> 3)], m & 7)};
    asm volatile("" : "+v"(wt[m]));
  }
  const int sg = (l >> 2) & 3;
  STAMP(21);
  SB();

  {
    const int po = WS_PO + ((br * 32 + h) * 64 + (l & 3) * 16 + sg) * 512 + (16 * w + 4 * g) * 4;
    v4u pv[4];
#pragma unroll
    for (int n = 0; n < 4; ++n) pv[n] = __builtin_amdgcn_raw_buffer_load_b128(rs, po + n * 2048, 0, AUX_SC1);
    SB();
    lds_wait(L, S_W, 1);
    const f32x4 wg = *(const LAS f32x4*)(L + L_WGT + ((l & 3) * 16 + sg * 4) * 4);
    SB();
    for (unsigned spin = 0;; ++spin) {
      const bool bad = has_sent(pv[0]) | has_sent(pv[1]) | has_sent(pv[2]) | has_sent(pv[3]);
      if (__builtin_amdgcn_ballot_w64(bad) == 0ull || spin > SPIN_CAP) break;
      asm volatile("" ::: "memory");
      if (bad) {
#pragma unroll
        for (int n = 0; n < 4; ++n) pv[n] = __builtin_amdgcn_raw_buffer_load_b128(rs, po + n * 2048, 0, AUX_SC1);
      }
    }
    STAMP(8);
    f32x4 o = __builtin_bit_cast(f32x4, pv[0]) * wg.x + __builtin_bit_cast(f32x4, pv[1]) * wg.y +
              (__builtin_bit_cast(f32x4, pv[2]) * wg.z + __builtin_bit_cast(f32x4, pv[3]) * wg.w);
#pragma unroll
    for (int k = 0; k < 4; ++k) { float t = o[k]; t += dpp<ROR8>(t); t += dpp<ROR4>(t); o[k] = t; }
    const unsigned o01 = pk_bf16(o.x, o.y), o23 = pk_bf16(o.z, o.w);
    v2u bm[4];
#pragma unroll
    for (int k = 0; k < 4; ++k) bm[k] = v2u{sg == k ? o01 : 0u, sg == k ? o23 : 0u};
    LAS f32x4* d = (LAS f32x4*)(L + L_RED2 + (w * 4 + (l & 3)) * 1056 + g * 128 + sg * 16);
#pragma unroll
    for (int a = 0; a < 4; ++a) {
      f32x4 z = {0.f, 0.f, 0.f, 0.f};
#pragma unroll
      for (int k = 0; k < 4; ++k) z = mfma16(wt[4 * a + k], bm[k], z);
      d[(a >> 1) * 32 + (a & 1) * 4] = z;
    }
  }
  STAMP(18);
  lds_barrier();
  if (tid < 256) {
    const int bb = tid >> 6, quad = tid & 63;
    const LAS char* src = L + L_RED2 + bb * 1056 + quad * 16;
    f32x4 a = *(const LAS f32x4*)src;
#pragma unroll
    for (int ww = 1; ww < 8; ++ww) a += *(const LAS f32x4*)(src + ww * (4 * 1056));
    const int cons = (br * 4 + cq) * 32 + bb * 8 + (quad >> 3);
    __builtin_amdgcn_raw_buffer_store_b128(nosent(a), rs, WS_OUT + cons * 4096 + h * 128 + (quad & 7) * 16, 0, AUX_SC1);
  }
  STAMP(9);
  if (w == 7) {
    const int io = WS_OUT + idx * 4096 + l * 16;
    v4u pv[4];
    for (unsigned spin = 0;; ++spin) {
      asm volatile("" ::: "memory");
#pragma unroll
      for (int n = 0; n < 4; ++n) pv[n] = __builtin_amdgcn_raw_buffer_load_b128(rs, io + n * 1024, 0, AUX_SC1);
      const bool bad = has_sent(pv[0]) | has_sent(pv[1]) | has_sent(pv[2]) | has_sent(pv[3]);
      if (__builtin_amdgcn_ballot_w64(bad) == 0ull || spin > SPIN_CAP) break;
      __builtin_amdgcn_s_sleep(1);
    }
    f32x4 a = (__builtin_bit_cast(f32x4, pv[0]) + __builtin_bit_cast(f32x4, pv[1])) +
              (__builtin_bit_cast(f32x4, pv[2]) + __builtin_bit_cast(f32x4, pv[3]));
#pragma unroll
    for (int k = 0; k < 4; ++k) { float t = a[k]; t += dpp<ROR8>(t); a[k] = x32_sum(x16_sum(t)); }
    const int r = (l >> 3) & 3;
    const float v = r == 0 ? a.x : r == 1 ? a.y : r == 2 ? a.z : a.w;
    const int obr = idx >> 7, ocq = (idx >> 5) & 3, ob = (idx >> 3) & 3, oc = idx & 7;
    if (l < 32) out[ob * 2048 + obr * 1024 + ocq * 256 + oc * 32 + l] = (unsigned short)(pk_bf16(v, v) & 0xFFFFu);
    STAMP(10);
  }
  STAMP(22);
  STAMP_FLUSH();
}

extern "C" void kernel_launch(void* const* d_in, const int* in_sizes, int n_in, void* d_out, int out_size, void* d_ws,
                              size_t ws_size, hipStream_t stream) {
  hipLaunchKernelGGL(fused_decode, dim3(256), dim3(576), 0, stream, (const char*)d_in[0], (const char*)d_in[1],
                     (const char*)d_in[2], (const char*)d_in[3], (const char*)d_in[4], (const char*)d_in[5],
                     (const char*)d_in[6], (const char*)d_in[7], (const char*)d_in[8], (const char*)d_in[9],
                     (unsigned short*)d_out, (char*)d_ws);
}
```

```cpp
#include <hip/hip_runtime.h>
#include <stdint.h>

#define TAU 290
#define WIN 5
#define XPT 8
#define KBPOS 8
#define Q1DELAY 80
#define Q2DELAY 80
#define TQ 850
#define MLDELAY 170
#define H1DELAY (200 + (13 - XPT) * TAU / 10)
#define H1DELAYB 180
#define H2DELAY 50
#define H2AT 160

typedef unsigned v4u __attribute__((ext_vector_type(4)));
typedef unsigned v2u __attribute__((ext_vector_type(2)));
typedef float f32x4 __attribute__((ext_vector_type(4)));
typedef __bf16 bf16x8 __attribute__((ext_vector_type(8)));
typedef short s16x4 __attribute__((ext_vector_type(4)));
#define GAS __attribute__((address_space(1)))
#define LAS __attribute__((address_space(3)))
#define SB() __builtin_amdgcn_sched_barrier(0)
#define F4(x) __builtin_bit_cast(f32x4, x)
#define UNLIKELY(x) __builtin_expect((x), 0)

constexpr int WS_EPOCH = 0;
constexpr int WS_QP = 4096;
constexpr int WS_QF = WS_QP + 256 * 2048;
constexpr int WS_ML = WS_QF + 2 * 4 * 4 * 1024 * 4;
constexpr int WS_PO = WS_ML + 2 * 32 * 64 * 16;
constexpr int WS_OUT = WS_PO + 2 * 32 * 64 * 128 * 4;
constexpr int WS_END = WS_OUT + 256 * 64 * 32 * 4;
constexpr int AUX_NT = 2, AUX_SC1 = 16, OOB = 0x7FFFFFF0;
constexpr unsigned SPIN_CAP = 1u << 18;

constexpr int L_SYNC = 0;
constexpr int L_WGT = 64;
constexpr int L_QRED = 512;
constexpr int L_QT = L_QRED + 8 * 4 * 544;
constexpr int L_QS = L_QT + 2048;
constexpr int L_MX = L_QS + 2 * 8 * 272;
constexpr int L_LS = L_MX + 512;
constexpr int L_RED = L_LS + 512;
constexpr int L_OB = L_RED + 2 * 8 * 8 * 528;
constexpr int L_END = L_OB + 512 + 16;
constexpr int S_QP = 0, S_Q = 4, S_MX = 8, S_V = 12, S_W = 16, S_R = 20, S_O = 24, S_EP = 32;

#define MKRS(ptr, n) __builtin_amdgcn_make_buffer_rsrc((void*)(ptr), 0, (n), 0x00020000)
#define LDNT(rs, voff, soff) __builtin_amdgcn_raw_buffer_load_b128((rs), (voff), (soff), AUX_NT)
__device__ __forceinline__ f32x4 mfma32(v4u a, v4u b, f32x4 c) {
  return __builtin_amdgcn_mfma_f32_16x16x32_bf16(__builtin_bit_cast(bf16x8, a), __builtin_bit_cast(bf16x8, b), c, 0, 0, 0);
}
__device__ __forceinline__ f32x4 mfma16(v2u a, v2u b, f32x4 c) {
  return __builtin_amdgcn_mfma_f32_16x16x16bf16_1k(__builtin_bit_cast(s16x4, a), __builtin_bit_cast(s16x4, b), c, 0, 0, 0);
}
__device__ __forceinline__ unsigned pk_bf16(float lo, float hi) {
  typedef __bf16 bf16x2 __attribute__((ext_vector_type(2)));
  typedef float f32x2 __attribute__((ext_vector_type(2)));
  f32x2 v = {lo, hi};
  return __builtin_bit_cast(unsigned, __builtin_convertvector(v, bf16x2));
}
__device__ __forceinline__ unsigned tr2(const v4u& r0, const v4u& r1, int dd) {
  return __builtin_amdgcn_perm(r1[dd >> 1], r0[dd >> 1], (dd & 1) ? 0x07060302u : 0x05040100u);
}
__device__ __forceinline__ float xg_max(float x) {
  auto s = __builtin_amdgcn_permlane16_swap(__float_as_uint(x), __float_as_uint(x), false, false);
  x = fmaxf(__uint_as_float(s[0]), __uint_as_float(s[1]));
  auto t = __builtin_amdgcn_permlane32_swap(__float_as_uint(x), __float_as_uint(x), false, false);
  return fmaxf(__uint_as_float(t[0]), __uint_as_float(t[1]));
}
__device__ __forceinline__ float x16_sum(float x) {
  auto s = __builtin_amdgcn_permlane16_swap(__float_as_uint(x), __float_as_uint(x), false, false);
  return __uint_as_float(s[0]) + __uint_as_float(s[1]);
}
__device__ __forceinline__ float x32_sum(float x) {
  auto t = __builtin_amdgcn_permlane32_swap(__float_as_uint(x), __float_as_uint(x), false, false);
  return __uint_as_float(t[0]) + __uint_as_float(t[1]);
}
template <int CTRL> __device__ __forceinline__ float dpp(float x) {
  return __builtin_bit_cast(float, __builtin_amdgcn_mov_dpp(__builtin_bit_cast(int, x), CTRL, 0xf, 0xf, true));
}
constexpr int ROR1 = 0x121, ROR2 = 0x122, ROR4 = 0x124, ROR8 = 0x128;
__device__ __forceinline__ float max8(f32x4 a, f32x4 b) {
  return fmaxf(fmaxf(fmaxf(a.x, a.y), fmaxf(a.z, a.w)), fmaxf(fmaxf(b.x, b.y), fmaxf(b.z, b.w)));
}

__device__ __forceinline__ unsigned long long launch_tag(unsigned long long epoch) { return (epoch + 1ull) * 0x9E3779B97F4A7C15ull; }
__device__ __forceinline__ v4u tag_bytes(unsigned long long tag, int parity) {
  const unsigned t = parity ? (unsigned)(tag >> 32) : (unsigned)tag;
  return v4u{t & 0xFFu, (t >> 8) & 0xFFu, (t >> 16) & 0xFFu, t >> 24};
}
__device__ __forceinline__ v4u tagged(f32x4 v, v4u tb) { return (__builtin_bit_cast(v4u, v) & 0xFFFFFF00u) | tb; }
__device__ __forceinline__ unsigned or4(v4u u) { return (u.x | u.y) | (u.z | u.w); }
__device__ __forceinline__ bool any_lane(bool x) { return __builtin_amdgcn_ballot_w64(x) != 0ull; }
template <int N> __device__ __forceinline__ void waitvm() { asm volatile("s_waitcnt vmcnt(%0)" ::"i"(N) : "memory"); }
__device__ __forceinline__ unsigned now() { return (unsigned)__builtin_amdgcn_s_memrealtime(); }
__device__ __forceinline__ void twait(unsigned tgt) {
  while ((int)(now() - tgt) < 0) __builtin_amdgcn_s_sleep(1);
}

template <int N, int S4, int S8> __device__ __forceinline__ unsigned read_pass(v4u (&pv)[N], __amdgpu_buffer_rsrc_t rs, int& off, v4u tg) {
  asm volatile("" : "+v"(off)::"memory");
#pragma unroll
  for (int n = 0; n < N; ++n) pv[n] = __builtin_amdgcn_raw_buffer_load_b128(rs, off + (n & 3) * 1024, ((n >> 2) & 1) * S4 + (n >> 3) * S8, AUX_SC1);
  SB();
  unsigned any = 0;
#pragma unroll
  for (int n = 0; n < N; ++n) { pv[n] ^= tg; any |= or4(pv[n]); }
  return any;
}
__device__ __forceinline__ bool again(bool stale, unsigned& spin) { return UNLIKELY(any_lane(stale)) && ++spin < SPIN_CAP; }
__device__ __forceinline__ void lds_signal(LAS char* L, int word) {
  const unsigned a = (unsigned)(size_t)(L + L_SYNC + word), one = 1u;
  asm volatile("s_mov_b64 exec, 1\n\tds_add_u32 %0, %1\n\ts_mov_b64 exec, -1" ::"v"(a), "v"(one) : "memory");
}
template <bool SLEEP> __device__ __forceinline__ void lds_wait(LAS char* L, int word, unsigned want) {
  while (__builtin_amdgcn_readfirstlane(__hip_atomic_load((LAS unsigned*)(L + L_SYNC + word), __ATOMIC_RELAXED, __HIP_MEMORY_SCOPE_WORKGROUP)) < want)
    if (SLEEP) __builtin_amdgcn_s_sleep(1);
  asm volatile("" ::: "memory");
}


__global__ void __launch_bounds__(576, 3)
fused_decode(const char* __restrict__ xA, const char* __restrict__ kA, const char* __restrict__ vA, const char* __restrict__ wqA,
             const char* __restrict__ woA, const char* __restrict__ xB, const char* __restrict__ kB, const char* __restrict__ vB,
             const char* __restrict__ wqB, const char* __restrict__ woB, unsigned short* __restrict__ out, char* ws) {
  __shared__ __attribute__((aligned(16))) char smem[L_END];
  LAS char* L = (LAS char*)smem;
  const int c = blockIdx.x, tid = threadIdx.x, l = tid & 63, w = __builtin_amdgcn_readfirstlane(tid >> 6);
  const int kvh = c >> 6, gi = (c & 7) * 8 + ((c >> 3) & 7), idx = kvh * 64 + gi;
  const int br = gi >> 5, cb = gi & 31;
  const int h = kvh * 8 + ((gi >> 2) & 7), dh = (gi >> 1) & 1, ch = gi & 1;
  const int ia = (c & 7) * 32 + (c >> 3), kva = ia >> 6, b = (ia >> 4) & 3, s = ia & 15;
  const int g = l >> 4, rho = l & 15;
  const __amdgpu_buffer_rsrc_t rs = MKRS(ws, WS_END);

  const int qrow = 256 * (cb & 3) + 32 * w + 8 * g;
  const __amdgpu_buffer_rsrc_t rwq = MKRS(br ? wqB : wqA, w < 8 ? 8 << 20 : 0);
  const int wqp = qrow * 8192 + (kvh * 1024 + (cb >> 2) * 128) * 2 + rho * 16;
  v4u wr[8];
  wr[0] = LDNT(rwq, wqp, 0);
  SB();
  const unsigned t_entry = now();
  SB();
#pragma unroll
  for (int n = 1; n < 4; ++n) wr[n] = LDNT(rwq, wqp, n * 8192);
  SB();
  if (tid >= 512 && l < 12) *(LAS unsigned*)(L + (l < 8 ? L_SYNC + l * 4 : L_OB + 512 - 32 + l * 4)) = 0u;
  asm volatile("s_waitcnt lgkmcnt(0)\n\ts_barrier" ::: "memory");
  SB();
  constexpr float C2 = 0.08838834764831845f * 1.4426950408889634f;

  if (w == 8) {
    __builtin_amdgcn_s_setprio(3);
    const unsigned long long epoch =
        __hip_atomic_load((GAS unsigned long long*)(ws + WS_EPOCH), __ATOMIC_RELAXED, __HIP_MEMORY_SCOPE_AGENT);
    unsigned spin = 0;
    const unsigned long long tag = launch_tag(epoch);
    const v4u tg = tag_bytes(tag, l & 1);
    const v4u t16 = {(unsigned)tag & 0xFFFFu, (unsigned)tag >> 16, (unsigned)(tag >> 32) & 0xFFFFu, (unsigned)(tag >> 48)};
    if (l == 0) *(LAS unsigned long long*)(L + L_SYNC + S_EP) = epoch;
    lds_wait<true>(L, S_QP, 8);
    {
      const LAS char* src = L + L_QRED + (l >> 5) * 544 + (l & 31) * 16;
      f32x4 a0 = *(const LAS f32x4*)src, a1 = *(const LAS f32x4*)(src + 2 * 544);
#pragma unroll
      for (int n = 1; n < 8; ++n) { a0 += *(const LAS f32x4*)(src + n * 4 * 544); a1 += *(const LAS f32x4*)(src + n * 4 * 544 + 2 * 544); }
      LAS float* t = (LAS float*)(L + L_QT) + (l >> 3) * 32 + (l & 7);
      t[0] = a0.x; t[8] = a0.y; t[16] = a0.z; t[24] = a0.w;
      t[256] = a1.x; t[264] = a1.y; t[272] = a1.z; t[280] = a1.w;
      asm volatile("s_waitcnt lgkmcnt(0)" ::: "memory");
      a0 = *((const LAS f32x4*)(L + L_QT) + l); a1 = *((const LAS f32x4*)(L + L_QT) + 64 + l);
      const int qslot = WS_QP + ((idx & ~3) + ((l >> 3) & 3)) * 2048 + (cb & 3) * 512 + (l >> 5) * 128 + (l & 7) * 16;
      __builtin_amdgcn_raw_buffer_store_b128(tagged(a0, tg), rs, qslot, 0, AUX_SC1);
      __builtin_amdgcn_raw_buffer_store_b128(tagged(a1, tg), rs, qslot + 256, 0, AUX_SC1);
    }
    twait(now() + Q1DELAY);
    {
      v4u pq[2];
      int o1 = WS_QP + idx * 2048 + l * 16;
      while (again((read_pass<2, 0, 0>(pq, rs, o1, tg) & 0xFFu) != 0u, spin)) __builtin_amdgcn_s_sleep(4);
      f32x4 q4 = F4(pq[0]) + F4(pq[1]);
#pragma unroll
      for (int k = 0; k < 4; ++k) q4[k] = x32_sum(q4[k]);
      const unsigned p01 = pk_bf16(q4.x, q4.y), p23 = pk_bf16(q4.z, q4.w);
      __builtin_amdgcn_raw_buffer_store_b128(v4u{p01 << 16, p01 & 0xFFFF0000u, p23 << 16, p23 & 0xFFFF0000u} | t16, rs,
                                             l < 32 ? WS_QF + ((br * 4 + kvh) * 4 + (l >> 3)) * 4096 + cb * 128 + (l & 7) * 16 : OOB, 0, AUX_SC1);
    }
    twait(now() + Q2DELAY);
    twait(t_entry + TQ);
    {
      v4u qg[8];
      int qo = WS_QF + (kva * 4 + b) * 4096 + l * 16;
      while (again((read_pass<8, 65536, 0>(qg, rs, qo, t16) & 0xFFFFu) != 0u, spin)) __builtin_amdgcn_s_sleep(4);
#pragma unroll
      for (int n = 0; n < 8; ++n)
        *(LAS v2u*)(L + L_QS + (2 * n + (l >> 5)) * 272 + (l & 31) * 8) = v2u{(qg[n].x >> 16) | qg[n].y, (qg[n].z >> 16) | qg[n].w};
    }
    lds_signal(L, S_Q);
    lds_wait<true>(L, S_V, 8);
    const unsigned tml = now();
    const v4u tgm = {t16.x, t16.y, t16.z & 0xFFu, t16.w};
    {
      const LAS f32x4* ls = (const LAS f32x4*)(L + L_LS + rho * 32);
      const LAS f32x4* mx = (const LAS f32x4*)(L + L_MX + rho * 32);
      const f32x4 l0 = ls[0], l1 = ls[1];
      const unsigned ul = __float_as_uint(((l0.x + l0.y) + (l0.z + l0.w)) + ((l1.x + l1.y) + (l1.z + l1.w)));
      const unsigned um = __float_as_uint(max8(mx[0], mx[1]) * C2);
      __builtin_amdgcn_raw_buffer_store_b128(v4u{um & 0xFFFF0000u, um << 16, ul & 0xFFFFFF00u, tml << 16} | tgm, rs,
                                             l < 16 ? WS_ML + (((l >> 3) * 32 + kva * 8 + (l & 7)) * 64 + (ia & 63)) * 16 : OOB, 0, AUX_SC1);
    }
#pragma unroll
    for (int n = 0; n < 4; ++n) {
      const LAS char* src = L + L_RED + 8 * 8 * 528 + (2 * n + (l >> 5)) * 528 + (l & 31) * 16;
      f32x4 a = *(const LAS f32x4*)src;
#pragma unroll
      for (int ww = 1; ww < 8; ++ww) a += *(const LAS f32x4*)(src + ww * (8 * 528));
      __builtin_amdgcn_raw_buffer_store_b128(tagged(a, tg), rs, WS_PO + ((32 + kva * 8 + 2 * n + (l >> 5)) * 64 + (ia & 63)) * 512 + (l & 31) * 16, 0, AUX_SC1);
    }
    twait(tml + MLDELAY);
    unsigned tlast;
    {
      int mo = WS_ML + ((br * 32 + h) * 64 + l) * 16;
      v4u pr[1];
      for (;;) {
        read_pass<1, 0, 0>(pr, rs, mo, tgm);
        if (!again((((pr[0].x | pr[0].y | pr[0].w) & 0xFFFFu) | (pr[0].z & 0xFFu)) != 0u, spin)) break;
        __builtin_amdgcn_s_sleep(8);
      }
      const v4u r = pr[0];
      float late = (float)((int)(r.w - (tml << 16)) >> 16);
      late = fmaxf(late, dpp<ROR8>(late)); late = fmaxf(late, dpp<ROR4>(late)); late = fmaxf(late, dpp<ROR2>(late));
      late = xg_max(fmaxf(late, dpp<ROR1>(late)));
      tlast = tml + (unsigned)min(__builtin_amdgcn_readfirstlane((int)late), 1000);
      const float m = __uint_as_float(r.x | (r.y >> 16)), ls = __uint_as_float(r.z);
      float M = m;
      M = fmaxf(M, dpp<ROR8>(M)); M = fmaxf(M, dpp<ROR4>(M)); M = fmaxf(M, dpp<ROR2>(M)); M = fmaxf(M, dpp<ROR1>(M));
      const float e = __builtin_amdgcn_exp2f(m - M);
      float Ls = e * ls;
      Ls += dpp<ROR8>(Ls); Ls += dpp<ROR4>(Ls); Ls += dpp<ROR2>(Ls); Ls += dpp<ROR1>(Ls);
      *(LAS float*)(L + L_WGT + ((l >> 4) * 16 + (l & 1) * 8 + ((l >> 1) & 7)) * 4) = e / Ls;
    }
    asm volatile("s_waitcnt lgkmcnt(0)" ::: "memory");
    const f32x4 wg0 = *(const LAS f32x4*)(L + L_WGT + (l >> 3) * 32), wg1 = *(const LAS f32x4*)(L + L_WGT + (l >> 3) * 32 + 16);
    int po = WS_PO + ((br * 32 + h) * 64 + (l >> 4) * 16 + ((l >> 3) & 1)) * 512 + dh * 256 + (l & 7) * 16;
    const int ci = br * 128 + kvh * 32 + cb;
    int io = WS_OUT + ci * 8192 + l * 16, ob = L_OB + (l >> 4) * 128 + (l & 7) * 8;
    int oo = l < 32 ? ((l & 3) * 2048 + br * 1024 + ((ci >> 3) & 15) * 64 + (ci & 1) * 32 + 8 * (l >> 3) + 4 * ((ci >> 2) & 1) + (ci & 2) + ((l >> 2) & 1)) * 2 : OOB;
    asm volatile("" : "+v"(po), "+v"(io), "+v"(ob), "+v"(oo));
    twait(tlast + (br ? H1DELAYB : H1DELAY));
    {
      f32x4 o[2];
      for (;;) {
        v4u pv[16];
        const unsigned any = read_pass<16, 4096, 128>(pv, rs, po, tg);
#pragma unroll
        for (int e = 0; e < 2; ++e) {
          o[e] = F4(pv[8 * e]) * wg0.x; o[e] += F4(pv[8 * e + 1]) * wg0.y; o[e] += F4(pv[8 * e + 2]) * wg0.z; o[e] += F4(pv[8 * e + 3]) * wg0.w;
          o[e] += F4(pv[8 * e + 4]) * wg1.x; o[e] += F4(pv[8 * e + 5]) * wg1.y; o[e] += F4(pv[8 * e + 6]) * wg1.z; o[e] += F4(pv[8 * e + 7]) * wg1.w;
          asm volatile("" : "+v"(o[e]));
        }
        if (!again((any & 0xFFu) != 0u, spin)) break;
      }
#pragma unroll
      for (int e = 0; e < 2; ++e) {
#pragma unroll
        for (int k = 0; k < 4; ++k) o[e][k] += dpp<ROR8>(o[e][k]);
        *(LAS v2u*)(L + ob + e * 64) = v2u{pk_bf16(o[e].x, o[e].y), pk_bf16(o[e].z, o[e].w)};
      }
    }
    lds_signal(L, S_W);
    lds_wait<true>(L, S_O, 8);
    twait(now() + H2DELAY);
    twait(tlast + H1DELAY + H2AT);
    {
      f32x4 t;
      for (;;) {
        v4u pv[8];
        const unsigned any = read_pass<8, 4096, 0>(pv, rs, io, tg);
        t = ((F4(pv[0]) + F4(pv[1])) + (F4(pv[2]) + F4(pv[3]))) + ((F4(pv[4]) + F4(pv[5])) + (F4(pv[6]) + F4(pv[7])));
        asm volatile("" : "+v"(t));
        if (!again((any & 0xFFu) != 0u, spin)) break;
      }
      const bool b3 = (l >> 3) & 1;
      const float u0 = (b3 ? t.y : t.x) + dpp<ROR8>(b3 ? t.x : t.y), u1 = (b3 ? t.w : t.z) + dpp<ROR8>(b3 ? t.z : t.w);
      const auto sw = __builtin_amdgcn_permlane16_swap(__float_as_uint(u0), __float_as_uint(u1), false, false);
      const float v = x32_sum(__uint_as_float(sw[0]) + __uint_as_float(sw[1]));
      __builtin_amdgcn_raw_buffer_store_b16((unsigned short)pk_bf16(v, v), MKRS(out, 16384), oo, 0, 0);
    }
    const unsigned long long e1 = epoch + 1ull;
    __builtin_amdgcn_raw_buffer_store_b64(v2u{(unsigned)e1, (unsigned)(e1 >> 32)}, rs, (idx | l) == 0 ? WS_EPOCH : OOB, 0, AUX_SC1);
    asm volatile(".Lcodepf_end1:");
    return;
  }

  const unsigned tb = t_entry + (unsigned)w;
  const int sg = (l >> 2) & 3;
  const v4u xf = *(const GAS v4u*)((br ? xB : xA) + ((l & 3) * 1024 + qrow) * 2);
  unsigned cpf;
  {
    unsigned long long pc; int len;
    asm volatile("s_getpc_b64 %0\n.Lcodepf_here:\n\ts_mov_b32 %1, 0\n\t"
                 "s_max_i32 %1, %1, .Lcodepf_end1-.Lcodepf_here\n\ts_max_i32 %1, %1, .Lcodepf_end2-.Lcodepf_here"
                 : "=s"(pc), "=s"(len)::"scc");
    len = (int)(((((unsigned)pc + (unsigned)len) | 4095u) + 1u) - (unsigned)pc);
    cpf = __builtin_amdgcn_raw_buffer_load_b32(MKRS(pc, len), l == 0 ? ((c >> 3) * 8 + w) * 128 : OOB, 0, 0);
  }
  const int wbase = (b * 4 + kva) * (8192 * 256) + (128 * s + 16 * w) * 256;
  const int koff = wbase + (4 * ((rho >> 1) & 1) + (rho >> 2)) * 256 + g * 32 + 16 * (rho & 1);
  const int voff = wbase + l * 16;
  constexpr int TS = 2048 * 256;
  const int ru = b + kva;
  v4u kf[20], vf[20], wo[8];
  const __amdgpu_buffer_rsrc_t rkA = MKRS(kA, 32 << 20), rkB = MKRS(kB, 32 << 20), rvA = MKRS(vA, 32 << 20), rvB = MKRS(vB, 32 << 20);
#define KOFF(n) (koff + ((n) & 1) * 128 + (((n) >> 1) & 1) * 2048)
#define KLOAD(n) ((n) < 16 ? LDNT(rkA, KOFF(n), ((((n) >> 2) + ru) & 3) * TS) : LDNT(rkB, KOFF(n), 3 * TS))
#define VLOAD(n) ((n) >= 4 ? LDNT(rvA, voff + ((n) & 3) * 1024, ((((n) >> 2) - 1 + ru) & 3) * TS) : LDNT(rvB, voff + ((n) & 3) * 1024, 3 * TS))
#define SLOT(n, win) do { SB(); waitvm<(win) - 1>(); twait(tb + (unsigned)((n) * TAU / 10)); } while (0)
#define KI(n) ((n) < KBPOS ? (n) : (n) < KBPOS + 4 ? 16 + (n) - KBPOS : (n) - 4)
#define PK(n) do { SLOT(8 + (n), WIN); kf[KI(n)] = KLOAD(KI(n)); SB(); } while (0)
#define PV(n) do { SLOT(28 + (n), WIN); vf[n] = VLOAD(n); SB(); } while (0)
#pragma unroll
  for (int n = 4; n < 8; ++n) { SLOT(n, WIN + 2); wr[n] = LDNT(rwq, wqp, n * 8192); SB(); }
  PK(0);
  PK(1);
  {
    v4u xm[4];
#pragma unroll
    for (int k = 0; k < 4; ++k) { xm[k] = sg == k ? xf : v4u{0u, 0u, 0u, 0u}; asm volatile("" : "+v"(xm[k])); }
    LAS f32x4* d = (LAS f32x4*)(L + L_QRED + (w * 4 + (l & 3)) * 544 + g * 128 + sg * 16);
#pragma unroll
    for (int a = 0; a < 2; ++a) {
      PK(2 + a);
      f32x4 z = {0.f, 0.f, 0.f, 0.f};
#pragma unroll
      for (int k = 0; k < 4; ++k) {
        const int dd = 4 * a + k;
        z = mfma32(v4u{tr2(wr[0], wr[1], dd), tr2(wr[2], wr[3], dd), tr2(wr[4], wr[5], dd), tr2(wr[6], wr[7], dd)}, xm[k], z);
      }
      d[a * 4] = z;
    }
  }
  asm volatile("" ::"v"(cpf));
  lds_signal(L, S_QP);
#pragma unroll
  for (int n = 4; n < 20; ++n) PK(n);
#pragma unroll
  for (int n = 0; n < XPT; ++n) PV(n);
  lds_wait<true>(L, S_Q, 1);
  const unsigned long long epoch = *(const LAS unsigned long long*)(L + L_SYNC + S_EP);
  PV(XPT);

  f32x4 sc[5];
  {
    const LAS char* qp = L + L_QS + (l & 7) * 272 + g * 32 + 16 * (rho >> 3);
    const v4u qa0 = *(const LAS v4u*)qp, qa1 = *(const LAS v4u*)(qp + 128);
    const v4u qb0 = *(const LAS v4u*)(qp + 8 * 272), qb1 = *(const LAS v4u*)(qp + 8 * 272 + 128);
#pragma unroll
    for (int u = 0; u < 5; ++u) {
      const f32x4 z = {0.f, 0.f, 0.f, 0.f};
      const f32x4 t1 = mfma32(kf[4 * u + 1], u < 4 ? qa1 : qb1, mfma32(kf[4 * u], u < 4 ? qa0 : qb0, z));
      const f32x4 t2 = mfma32(kf[4 * u + 3], u < 4 ? qa1 : qb1, mfma32(kf[4 * u + 2], u < 4 ? qa0 : qb0, z));
      sc[u] = f32x4{t1.x + dpp<ROR8>(t1.y), t1.z + dpp<ROR8>(t1.w), t2.x + dpp<ROR8>(t2.y), t2.z + dpp<ROR8>(t2.w)};
      asm volatile("" : "+v"(sc[u]));
      if (u == 1) PV(XPT + 1);
      if (u == 3) PV(XPT + 2);
    }
  }
  {
    const float mA = xg_max(fmaxf(max8(sc[0], sc[1]), max8(sc[2], sc[3]))), mB = xg_max(max8(sc[4], sc[4]));
    if (l < 8) {
      *(LAS float*)(L + L_MX + (l * 8 + w) * 4) = mA;
      *(LAS float*)(L + L_MX + 256 + (l * 8 + w) * 4) = mB;
    }
  }
  lds_signal(L, S_MX);
  PV(XPT + 3);
  lds_wait<true>(L, S_MX, 8);
  PV(XPT + 4);
  unsigned pl[10], ph[10];
  {
    const LAS f32x4* m = (const LAS f32x4*)(L + L_MX + (l & 7) * 32);
    const float MA = max8(m[0], m[1]) * C2, MB = max8(m[16], m[17]) * C2;
    float la = 0.f, lb = 0.f;
#pragma unroll
    for (int u = 0; u < 5; ++u) {
      const float M = u < 4 ? MA : MB;
      const float p0 = __builtin_amdgcn_exp2f(fmaf(sc[u].x, C2, -M)), p1 = __builtin_amdgcn_exp2f(fmaf(sc[u].y, C2, -M));
      const float p2 = __builtin_amdgcn_exp2f(fmaf(sc[u].z, C2, -M)), p3 = __builtin_amdgcn_exp2f(fmaf(sc[u].w, C2, -M));
      const float t = (p0 + p1) + (p2 + p3);
      if (u < 4) la += t; else lb = t;
      pl[2 * u] = rho < 8 ? pk_bf16(p0, p1) : 0u; pl[2 * u + 1] = rho < 8 ? pk_bf16(p2, p3) : 0u;
      ph[2 * u] = __builtin_amdgcn_mov_dpp(pl[2 * u], ROR8, 0xf, 0xf, true);
      ph[2 * u + 1] = __builtin_amdgcn_mov_dpp(pl[2 * u + 1], ROR8, 0xf, 0xf, true);
      asm volatile("" : "+v"(pl[2 * u]), "+v"(pl[2 * u + 1]), "+v"(ph[2 * u]), "+v"(ph[2 * u + 1]), "+v"(la));
      if (u == 1) PV(XPT + 5);
      if (u == 3) PV(XPT + 6);
    }
    la = x32_sum(x16_sum(la)); lb = x32_sum(x16_sum(lb));
    if (l < 8) {
      *(LAS float*)(L + L_LS + (l * 8 + w) * 4) = la;
      *(LAS float*)(L + L_LS + 256 + (l * 8 + w) * 4) = lb;
    }
  }
#define PV16(v0, pi, c) mfma16(v2u{tr2(vf[v0], vf[v0 + 1], q4 + 4), tr2(vf[v0 + 2], vf[v0 + 3], q4 + 4)}, v2u{ph[pi], ph[pi + 1]}, \
                        mfma16(v2u{tr2(vf[v0], vf[v0 + 1], q4), tr2(vf[v0 + 2], vf[v0 + 3], q4)}, v2u{pl[pi], pl[pi + 1]}, c))
  LAS f32x4* dA = (LAS f32x4*)(L + L_RED + (w * 8 + (l & 7)) * 528 + g * 128 + (rho >> 3) * 64);
#pragma unroll
  for (int q4 = 0; q4 < 4; ++q4) dA[8 * 8 * 33 + q4] = PV16(0, 8, (f32x4{0.f, 0.f, 0.f, 0.f}));
  lds_signal(L, S_V);
#pragma unroll
  for (int n = XPT + 7; n < 16; ++n) PV(n);
  f32x4 acc[4];
#pragma unroll
  for (int q4 = 0; q4 < 4; ++q4) {
    const v4u a0 = {tr2(vf[4], vf[5], q4), tr2(vf[6], vf[7], q4), tr2(vf[8], vf[9], q4), tr2(vf[10], vf[11], q4)};
    const v4u a1 = {tr2(vf[4], vf[5], q4 + 4), tr2(vf[6], vf[7], q4 + 4), tr2(vf[8], vf[9], q4 + 4), tr2(vf[10], vf[11], q4 + 4)};
    acc[q4] = mfma32(a1, v4u{ph[0], ph[1], ph[2], ph[3]}, mfma32(a0, v4u{pl[0], pl[1], pl[2], pl[3]}, f32x4{0.f, 0.f, 0.f, 0.f}));
    asm volatile("" : "+v"(acc[q4]));
    if (q4 == 1) PV(16);
  }
#pragma unroll
  for (int n = 17; n < 20; ++n) PV(n);
  const __amdgpu_buffer_rsrc_t rwo = MKRS(br ? woB : woA, 8 << 20);
  const int wop = (128 * h + 64 * dh + 32 * (rho >> 3) + 2 * g) * 2048 + 1024 * ch + 128 * w + (l & 7) * 16;
#define WLOAD(n) LDNT(rwo, wop, (8 * ((n) & 3) + ((n) >> 2)) * 2048)
#define PW(n, win) do { SLOT(48 + (n), win); wo[n] = WLOAD(n); SB(); } while (0)
#pragma unroll
  for (int q4 = 0; q4 < 4; ++q4) { acc[q4] = PV16(12, 4, acc[q4]); asm volatile("" : "+v"(acc[q4])); }
  v4u tgl = tag_bytes(launch_tag(epoch), l & 1);
  int mrg = L_RED + (w & 3) * (2 * 528) + (l >> 5) * 528 + (l & 31) * 16;
  int pst = WS_PO + ((kva * 8 + (w & 3) * 2 + (l >> 5)) * 64 + (ia & 63)) * 512 + (l & 31) * 16;
  asm volatile("" : "+v"(tgl), "+v"(mrg), "+v"(pst));
  PW(0, 3);
  PW(1, 3);
  waitvm<2>();
#pragma unroll
  for (int q4 = 0; q4 < 4; ++q4) dA[q4] = PV16(16, 6, acc[q4]);
  lds_signal(L, S_R);
  SB();
  wo[2] = WLOAD(2);
  SB();
  if (w < 4) {
    lds_wait<false>(L, S_R, 8);
    f32x4 a = *(const LAS f32x4*)(L + mrg);
#pragma unroll
    for (int ww = 1; ww < 8; ++ww) a += *(const LAS f32x4*)(L + mrg + ww * (8 * 528));
    __builtin_amdgcn_raw_buffer_store_b128(tagged(a, tgl), rs, pst, 0, AUX_SC1);
  }
#pragma unroll
  for (int n = 3; n < 8; ++n) PW(n, 16);

  {
    int oa[2], so[2];
#pragma unroll
    for (int k = 0; k < 2; ++k) {
      oa[k] = (sg & 1) == k ? L_OB + (l & 3) * 128 + (rho >> 3) * 64 + g * 16 : L_OB + 512;
      const int cid = ((br * 2 + ch) * 8 + w) * 8 + k * 4 + g;
      so[k] = rho < 8 ? WS_OUT + (cid * 64 + ((h * 2 + dh + cid) & 63)) * 128 + (l & 7) * 16 : OOB;
      asm volatile("" : "+v"(oa[k]), "+v"(so[k]));
    }
    unsigned wt[8][4];
#pragma unroll
    for (int y = 0; y < 4; ++y) {
#pragma unroll
      for (int dd = 0; dd < 8; ++dd) { wt[dd][y] = tr2(wo[2 * y], wo[2 * y + 1], dd); asm volatile("" : "+v"(wt[dd][y])); }
      SB();
    }
#define WT(dd) v4u{wt[dd][0], wt[dd][1], wt[dd][2], wt[dd][3]}
    lds_wait<false>(L, S_W, 1);
    const v4u bm0 = *(const LAS v4u*)(L + oa[0]), bm1 = *(const LAS v4u*)(L + oa[1]);
    f32x4 z[4];
#pragma unroll
    for (int a = 0; a < 4; ++a) z[a] = mfma32(WT(2 * a + 1), bm1, mfma32(WT(2 * a), bm0, f32x4{0.f, 0.f, 0.f, 0.f}));
#pragma unroll
    for (int a = 0; a < 2; ++a) {
      f32x4 y;
#pragma unroll
      for (int r = 0; r < 4; ++r) {
        auto sw = __builtin_amdgcn_permlane32_swap(__float_as_uint(z[2 * a][r]), __float_as_uint(z[2 * a + 1][r]), false, false);
        y[r] = __uint_as_float(sw[0]) + dpp<ROR8>(__uint_as_float(sw[1]));
      }
      __builtin_amdgcn_raw_buffer_store_b128(tagged(y, tgl), rs, so[a], 0, AUX_SC1);
    }
  }
  lds_signal(L, S_O);
  asm volatile(".Lcodepf_end2:");
}

extern "C" void kernel_launch(void* const* d_in, const int* in_sizes, int n_in, void* d_out, int out_size, void* d_ws,
                              size_t ws_size, hipStream_t stream) {
  hipLaunchKernelGGL(fused_decode, dim3(256), dim3(576), 0, stream, (const char*)d_in[0], (const char*)d_in[1],
                     (const char*)d_in[2], (const char*)d_in[3], (const char*)d_in[4], (const char*)d_in[5],
                     (const char*)d_in[6], (const char*)d_in[7], (const char*)d_in[8], (const char*)d_in[9],
                     (unsigned short*)d_out, (char*)d_ws);
}
```
